```python
import torch

def _softmax(x, axis=-1):
    return torch.softmax(x, dim=axis)
def _gelu(x):
    return torch.nn.functional.gelu(x, approximate="tanh")
def _silu(x):
    return torch.nn.functional.silu(x)
def _sigmoid(x):
    return torch.sigmoid(x)
def _layernorm(x, axis=-1, eps=1e-5):
    m = x.mean(dim=axis, keepdim=True)
    v = x.var(dim=axis, keepdim=True, unbiased=False)
    return (x - m) / torch.sqrt(v + eps)
def _scatter_add(tgt, idx, src, axis=0):
    return tgt.clone().index_add_(axis, idx, src)
def _segment_sum(src, seg, n):
    out = torch.zeros((n,) + src.shape[1:], dtype=src.dtype)
    return out.index_add_(0, seg, src)
def _pad(x, pw):
    flat = tuple(v for pair in reversed(pw) for v in pair)
    return torch.nn.functional.pad(x, flat)

kernel_name = 'concat_heads__mha_block__ssm_conv_s5__prompt'

def setup_inputs():

    _w = (lambda a: (lambda t: t.to(torch.bfloat16) if t.is_floating_point() else t)(torch.as_tensor(a)))
    def _A_setup():
        B, S, D, H = 1, 2048, 576, 32
        return {
            "x": _w((lambda *s: torch.randn(*s, dtype=torch.bfloat16) * 0.2)(B, S, D)),
            "wqkv": _w((lambda *s: torch.randn(*s, dtype=torch.bfloat16) * 0.2)(D, 3 * D)),
            "wo": _w((lambda *s: torch.randn(*s, dtype=torch.bfloat16) * 0.05)(D, D)),
        }
    _A_in = _A_setup()

    def _B_setup():
        B, S, D = 1, 2048, 576
        P = D
        Lambda_re = -0.5 * (lambda _x: torch.exp(torch.as_tensor(_x)))((lambda *s: torch.randn(*s, dtype=torch.bfloat16) * 0.8)(P))
        Lambda_im = (lambda *s: torch.randn(*s, dtype=torch.bfloat16) * 0.8)(P) * 1.0
        B_re = (lambda *s: torch.randn(*s, dtype=torch.bfloat16) * 0.8)(P, D) * (D ** -0.5)
        B_im = (lambda *s: torch.randn(*s, dtype=torch.bfloat16) * 0.8)(P, D) * (D ** -0.5)
        C_re = (lambda *s: torch.randn(*s, dtype=torch.bfloat16) * 0.8)(D, P) * (P ** -0.5)
        C_im = (lambda *s: torch.randn(*s, dtype=torch.bfloat16) * 0.8)(D, P) * (P ** -0.5)
        log_dt = (lambda *s: torch.randn(*s, dtype=torch.bfloat16) * 0.8)(P) * 0.3 - 2.0
        Dskip = (lambda *s: torch.randn(*s, dtype=torch.bfloat16) * 0.8)(D)
        return {
            "x": _w((lambda *s: torch.randn(*s, dtype=torch.bfloat16) * 0.05)(B, S, D)),
            "Lambda_re": _w(Lambda_re),
            "Lambda_im": _w(Lambda_im),
            "B_re": _w(B_re),
            "B_im": _w(B_im),
            "C_re": _w(C_re),
            "C_im": _w(C_im),
            "log_dt": _w(log_dt),
            "Dskip": _w(Dskip),
        }
    _B_in = _B_setup()

    return {
        "A_x": _A_in["x"],
        "A_wqkv": _A_in["wqkv"],
        "A_wo": _A_in["wo"],
        "B_x": _B_in["x"],
        "B_Lambda_re": _B_in["Lambda_re"],
        "B_Lambda_im": _B_in["Lambda_im"],
        "B_B_re": _B_in["B_re"],
        "B_B_im": _B_in["B_im"],
        "B_C_re": _B_in["C_re"],
        "B_C_im": _B_in["C_im"],
        "B_log_dt": _B_in["log_dt"],
        "B_Dskip": _B_in["Dskip"],
    }

def reference(A_x, A_wqkv, A_wo, B_x, B_Lambda_re, B_Lambda_im, B_B_re, B_B_im, B_C_re, B_C_im, B_log_dt, B_Dskip):
    def _A_ref(x, wqkv, wo):
        B, S, D = x.shape
        H = 32
        Dh = D // H
        qkv = (lambda x, *d: x.permute(*d))((x @ wqkv).reshape(B, S, 3, H, Dh), 2, 0, 3, 1, 4)
        q, k, v = qkv[0], qkv[1], qkv[2]
        scores = torch.einsum("bhqd,bhkd->bhqk", q, k) * (Dh ** -0.5)
        p = _softmax(scores, axis=-1)
        o = torch.einsum("bhqk,bhkd->bhqd", p, v)
        return (lambda x, *d: x.permute(*d))(o, 0, 2, 1, 3).reshape(B, S, D) @ wo

    def _B_ref(x, Lambda_re, Lambda_im, B_re, B_im, C_re, C_im, log_dt, Dskip):
        Bsz, L, H = x.shape
        Lam = Lambda_re + 1j * Lambda_im
        Bmat = B_re + 1j * B_im
        Cmat = C_re + 1j * C_im
        dt = torch.exp(log_dt)
        Lam_bar = torch.exp(dt * Lam)
        B_bar = ((Lam_bar - 1.0) / Lam)[:, None] * Bmat
        Bu = torch.einsum("bld,pd->blp", x + 0j, B_bar)
        h = torch.zeros_like(Bu[:, 0, :])
        hs = []
        for t in range(L):
            h = Lam_bar[None, :] * h + Bu[:, t, :]
            hs.append(h)
        hseq = torch.stack(hs, 1)
        y = 2.0 * torch.einsum("blp,dp->bld", hseq, Cmat).real + x * Dskip[None, None, :]
        return y

    a = _A_ref(A_x, A_wqkv, A_wo)
    b = _B_ref(B_x, B_Lambda_re, B_Lambda_im, B_B_re, B_B_im, B_C_re, B_C_im, B_log_dt, B_Dskip)
    return torch.concatenate([a, b], axis=-1)
```

```cpp
#include <hip/hip_runtime.h>
#include <stdint.h>

typedef float f32x2 __attribute__((ext_vector_type(2)));
typedef float f32x4 __attribute__((ext_vector_type(4)));
typedef float f32x32 __attribute__((ext_vector_type(32)));
typedef unsigned u32x2 __attribute__((ext_vector_type(2)));
typedef unsigned u32x4 __attribute__((ext_vector_type(4)));
typedef short s16x4 __attribute__((ext_vector_type(4)));
typedef unsigned u32x8 __attribute__((ext_vector_type(8)));
typedef unsigned u32x32 __attribute__((ext_vector_type(32)));
typedef unsigned long long u64;
#define AS1 __attribute__((address_space(1)))
#define AS3 __attribute__((address_space(3)))
#define DEV __device__ __forceinline__

constexpr unsigned QS = 27648;
constexpr unsigned WS_O = 256 * QS;
constexpr unsigned WS_H = WS_O + 2359296;
constexpr unsigned WS_WF = WS_H + 4718592;
constexpr unsigned WS_EG = WS_WF + 663552;
constexpr unsigned WS_FA = WS_EG + 156672;
constexpr unsigned WS_FH = WS_FA + 32768;
constexpr unsigned WS_FO = WS_FH + 32768;
constexpr unsigned WS_EP = WS_FO + 32768;

constexpr unsigned LDS_BYTES = 163840;
constexpr unsigned TS = 2336;
constexpr unsigned L_E16 = 32 + 29 * TS, L_Q = 32 + 25 * TS, L_G1 = 32 * TS, L_TOP = 64 * TS;
constexpr unsigned HB = 9280;
constexpr unsigned L_P = 155648;
constexpr unsigned L_W = 131072;
constexpr unsigned L_S = 98304;
constexpr int SPIN_CAP = 1 << 18;

struct Params {
  const char *xa, *wqkv, *wo, *xb, *lre, *lim, *bre, *bim, *cre, *cim, *ldt, *dsk;
  float* out;
  char* ws;
};

template <class T> DEV void lds_st(unsigned a, T v) { *(AS3 T*)a = v; }
DEV u32x2 lds_tr(unsigned a) { return __builtin_bit_cast(u32x2, __builtin_amdgcn_ds_read_tr16_b64_v4i16((AS3 s16x4*)a)); }
DEV u32x4 cat(u32x2 a, u32x2 b) { return u32x4{a.x, a.y, b.x, b.y}; }
DEV unsigned pkbf(float a, float b) {
  typedef __bf16 bf16x2 __attribute__((ext_vector_type(2)));
  return __builtin_bit_cast(unsigned, __builtin_convertvector(f32x2{a, b}, bf16x2));
}
DEV float asf(unsigned u) { return __builtin_bit_cast(float, u); }
DEV float bflo(unsigned u) { return __builtin_bit_cast(float, u << 16); }
DEV float bfhi(unsigned u) { return __builtin_bit_cast(float, u & 0xffff0000u); }
DEV void bar() { asm volatile("s_waitcnt lgkmcnt(0)\n s_barrier" ::: "memory"); }
template <int N> DEV void wait_vm() { asm volatile("s_waitcnt vmcnt(%0)" ::"n"(N) : "memory"); }
DEV void st16(char* base, unsigned off, u32x4 v) {
  asm volatile("global_store_dwordx4 %0, %1, %2 sc1\n s_nop 1" ::"v"(off), "v"(v), "s"(base) : "memory");
}
DEV void st8(char* base, unsigned off, u64 v) { __hip_atomic_store((AS1 u64*)(base + off), v, __ATOMIC_RELAXED, __HIP_MEMORY_SCOPE_AGENT); }
DEV u64 ld8(const char* base, unsigned off) { return __hip_atomic_load((const AS1 u64*)(base + off), __ATOMIC_RELAXED, __HIP_MEMORY_SCOPE_AGENT); }

DEV void dma4(const char* g, unsigned l, unsigned v0, unsigned v1, unsigned v2, unsigned v3) {
  asm volatile(
      "s_mov_b32 m0, %5\n s_nop 0\n"
      "global_load_lds_dwordx4 %0, %4\n"
      "global_load_lds_dwordx4 %1, %4 offset:1024\n"
      "global_load_lds_dwordx4 %2, %4 offset:2048\n"
      "global_load_lds_dwordx4 %3, %4 offset:3072\n" ::"v"(v0), "v"(v1), "v"(v2), "v"(v3), "s"(g), "s"(l) : "memory");
}
DEV void dma1(const char* g, unsigned l, unsigned v) {
  asm volatile("s_mov_b32 m0, %2\n s_nop 0\n global_load_lds_dwordx4 %0, %1\n" ::"v"(v), "s"(g), "s"(l) : "memory");
}
DEV void dma_tile(const char* g, unsigned l, u32x4 v) {
  asm volatile(
      "s_mov_b32 m0, %4\n s_nop 0\n"
      "global_load_lds_dwordx4 %0, %3 sc1\n"
      "global_load_lds_dwordx4 %1, %3 offset:1024 sc1\n"
      "global_load_lds_dword %2, %3 offset:2048 sc1\n" ::"v"(v.x), "v"(v.y), "v"(v.z), "s"(g), "s"(l) : "memory");
}
DEV u32x4 tile_addr(unsigned lane, unsigned s1, unsigned s2) {
  u32x4 v;
#pragma unroll
  for (int i = 0; i < 3; ++i) {
    const unsigned o = i < 2 ? lane * 16 + 1024 * i : lane * 4 + 2048, sg = ((o >> 4) * 57) >> 10;
    v[i] = o - 1024 * i + (sg >> 1) * (s2 - 576) + (sg & 1) * (s1 - 288);
  }
  v[3] = 0;
  return v;
}

template <int N> DEV void poll(const char* ws, const unsigned (&off)[N], u64 tag) {
#pragma nounroll
  for (int spin = 0; spin < SPIN_CAP; ++spin) {
    bool ok = true;
#pragma unroll
    for (int i = 0; i < N; ++i) ok &= ld8(ws, off[i]) == tag;
    if (__all(ok)) break;
    __builtin_amdgcn_s_sleep(4);
  }
  asm volatile("" ::: "memory");
}

DEV unsigned fslot(unsigned xcd, unsigned j, unsigned w) { return (j * 8 + w) * 128 + xcd * 8; }
struct Cx { float x, y; };
DEV Cx cmul(Cx a, Cx b) {
  Cx r;
  asm("v_mul_f32 %0, %2, %4\n v_mul_f32 %1, %2, %5\n v_fma_f32 %0, -%3, %5, %0\n v_fmac_f32 %1, %3, %4"
      : "=&v"(r.x), "=&v"(r.y) : "v"(a.x), "v"(a.y), "v"(b.x), "v"(b.y));
  return r;
}

struct Loader { unsigned x[4], w[4], b; };
DEV unsigned bslot(int s) { return ((0x000a07040d10ull >> (8 * (s >= 6 ? s - 6 : s))) & 255) << 13; }
DEV void issue_a(const Loader& L, const char* xg, const char* wg, int ks, int slot, int lw) {
  dma4(xg + ks * 128, slot * 32768 + lw * 4096, L.x[0], L.x[1], L.x[2], L.x[3]);
  dma4(wg + ks * (64 * 3456), slot * 32768 + 16384 + lw * 4096, L.w[0], L.w[1], L.w[2], L.w[3]);
}
DEV void issue_b(const Loader& L, const Params& P, int ks, int lw) {
  const unsigned sb = bslot(ks);
  dma4(P.xb + ks * 128, sb + lw * 4096, L.x[0], L.x[1], L.x[2], L.x[3]);
  dma1(P.bre + ks * 128, sb + 16384 + lw * 1024, L.b);
  dma1(P.bim + ks * 128, sb + 20480 + lw * 1024, L.b);
}
DEV unsigned perm32(int m) { return 16 * ((m >> 2) & 1) + (m & 3) + 4 * (m >> 3); }

DEV float exp_f(float x) {
  const float t = x * 1.44269502f, e = fmaf(x, 1.92596299e-8f, fmaf(x, 1.44269502f, -t)), r = __builtin_amdgcn_exp2f(t);
  return fmaf(r, e * 0.693147182f, r);
}
struct Raw { unsigned lr, li, dt; };
DEV Raw ssm_load(const Params& P, unsigned p) {
  Raw r;
  asm volatile("global_load_ushort %0, %3, %4\n global_load_ushort %1, %3, %5\n global_load_ushort %2, %3, %6"
               : "=&v"(r.lr), "=&v"(r.li), "=&v"(r.dt) : "v"(2 * p), "s"(P.lre), "s"(P.lim), "s"(P.ldt) : "memory");
  return r;
}
DEV Cx ssm_part1(Raw& r) {
  asm volatile("" : "+v"(r.lr), "+v"(r.li), "+v"(r.dt));
  const float dt = bflo(pkbf(exp_f(bflo(r.dt)), 0.f));
  const float ea = exp_f(dt * bflo(r.lr)), th = dt * bflo(r.li), rv = th * 0.15915494f;
  const float fr = __builtin_amdgcn_fractf(rv) + fmaf(th, 6.4206382e-9f, fmaf(th, 0.15915494f, -rv));
  Cx lam = {ea * __builtin_amdgcn_cosf(fr), -ea * __builtin_amdgcn_sinf(fr)};
  asm volatile("" : "+v"(lam.x), "+v"(lam.y));
  return lam;
}
DEV Cx ssm_part2(Raw r, Cx lam, unsigned la) {
  asm volatile("" : "+v"(r.lr), "+v"(r.li), "+v"(lam.x), "+v"(lam.y));
  const float lr = bflo(r.lr), li = bflo(r.li), nr = lam.x - 1.0f, ni = -lam.y;
  const bool big = fabsf(lr) >= fabsf(li);
  const float a = big ? lr : li, b = big ? li : lr, rat = b * __builtin_amdgcn_rcpf(a), inv = 2.0f * __builtin_amdgcn_rcpf(a + b * rat);
  const float fx = (big ? nr + ni * rat : nr * rat + ni) * inv, fy = (big ? ni - nr * rat : ni * rat - nr) * inv;
  Cx t = cmul(lam, lam);
  t = cmul(t, t); t = cmul(t, t); t = cmul(t, t);
  lds_st<f32x4>(la, f32x4{lam.x, lam.y, ni, fx});
  lds_st<f32x4>(la + 16, f32x4{-fx, -fy, t.x, t.y});
  return t;
}
DEV void ssm_part3(Cx t, int seg, unsigned la, unsigned lb) {
  asm volatile("" : "+v"(t.x), "+v"(t.y));
  const Cx one = {1.f, 0.f}, t32 = cmul(t, t), t64 = cmul(t32, t32), t128 = cmul(t64, t64), t256 = cmul(t128, t128);
  const Cx pw = cmul(cmul((seg & 1) ? t : one, (seg & 2) ? t32 : one), (seg & 4) ? t64 : one);
  lds_st<f32x4>(la + 32, f32x4{t128.x, t128.y, t256.x, t256.y});
  lds_st<f32x2>(lb, f32x2{pw.x, pw.y});
}
template <class T> DEV T pin(T x) { asm volatile("" : "+s"(x)); return x; }

__global__ void __launch_bounds__(512, 2) fused(Params P) {
  int lane = threadIdx.x & 63, h = lane >> 5;
  const int w = __builtin_amdgcn_readfirstlane(threadIdx.x >> 6), wq = w & 3, kh = w >> 2;
  const int xcd = blockIdx.x & 7, j = blockIdx.x >> 3;
  char* const ws = P.ws;
  const unsigned l16 = lane * 16;
  const bool isA = j < 28;
  u64 tag;
  if (w == 0) {
    unsigned sink, off = (j * 20 + min(lane, 19)) * 64;
    asm volatile("s_getpc_b64 s[40:41]\n .Lcode_from:\n v_min_u32 %1, .Lcode_to-.Lcode_from, %1\n global_load_dword %0, %1, s[40:41]\n"
                 : "={v243}"(sink), "+v"(off) :: "s40", "s41", "memory");
  }
  const int c_u = 4 * (xcd >> 1) + (j & 3), pg_m = 9 * (xcd & 1) + (j >> 2), pg_x = 9 * (xcd & 1) + 8;
  const int pg_u = kh ? pg_x : pg_m;
  const bool hasU = !kh || !isA;
  const int seg = 2 * wq + h;
  const int rt = c_u, ct = 7 * (xcd & 1) + (j >> 2);
  Loader L;
  f32x32 uu;
  const unsigned lpa = L_P + (lane & 31) * 48, lpb = L_P + 1536 + seg * 256 + (lane & 31) * 8;
  auto swz = [&](unsigned row) { return row * 1152 + (((lane & 7) ^ ((row >> 1) & 7)) << 4); };
  if (!hasU) {
    unsigned ch = (lane & 15) ^ ((lane >> 4) << 2);
    if (ct == 13) ch &= 7;
#pragma unroll
    for (int i = 0; i < 4; ++i) {
      L.x[i] = swz(c_u * 128 + 32 * wq + 8 * i + (lane >> 3)) - 1024 * i;
      L.w[i] = (16 * wq + 4 * i + (lane >> 4)) * 3456 + ct * 256 + ch * 16 - 1024 * i;
    }
    asm volatile("" : "+v"(L.x[0]), "+v"(L.x[1]), "+v"(L.x[2]), "+v"(L.x[3]), "+v"(L.w[0]), "+v"(L.w[1]), "+v"(L.w[2]), "+v"(L.w[3]));
    __builtin_amdgcn_sched_barrier(0);
    issue_a(L, P.xa, P.wqkv, 0, 1, wq);
    issue_a(L, P.xa, P.wqkv, 1, 2, wq);
    __builtin_amdgcn_sched_barrier(0);
  }

  u32x8 aa;
  Raw raw;
  const int wa = blockIdx.x - 224, wc3 = (wa * 43) >> 7, wst = 6 * (wa - 3 * wc3) + w;
  const bool hasW = w < 6 && wa < 27;
  const unsigned lx = wq * 4096 + kh * 2048, lb = 16384 + kh * 8192 + wq * 1024;
  auto issue_x = [&](int s) {
    const unsigned sb = ((s + 3) & 3) * 32768;
    dma1(P.xb + s * 128, sb + lx, aa[0]);
    dma1(P.xb + s * 128 + 1024, sb + lx + 1024, aa[1]);
    dma1(P.bre + s * 128, sb + lb, aa[2]);
    dma1(P.bim + s * 128, sb + lb + 4096, aa[2]);
  };
  if (__builtin_expect(!isA, 0)) {
    aa[0] = swz(c_u * 128 + 32 * wq + 16 * kh + (lane >> 3));
    aa[1] = swz(c_u * 128 + 32 * wq + 16 * kh + 8 + (lane >> 3)) - 1024;
    aa[2] = swz(32 * pg_u + 8 * wq + (lane >> 3));
    issue_x(0);
    raw = ssm_load(P, 32 * pg_u + (lane & 31));
  }
  const unsigned myf = fslot(xcd, j, w);

  const int head = xcd * 4 + (j >> 3), qb = j & 7, t32 = qb * 8 + wq * 2 + kh;
  const char* kvg = ws + 9216 + head * 288;
  const int u0 = (xcd * 234) >> 3, u1 = ((xcd + 1) * 234) >> 3;
  const bool hasT = u0 + j < u1;
  const int un = hasT ? u0 + j : u0;
  const int rt3 = un / 9, ct3 = un - rt3 * 9;
  const int t0 = min(80 * rt3, 1968);
  u32x32 sp;
  u32x8 mi, ta, tb;
  u32x4 wf1;
  auto fa = [](unsigned col, unsigned t64) {
    const unsigned c = col >> 7, hi = c >= 7;
    return WS_FA + fslot((t64 >> 3) * 2 + hi, (c - 7 * hi) * 4 + ((t64 >> 1) & 3), (t64 & 1) * 2 + ((col >> 6) & 1));
  };
  unsigned fo[3];
  u64 fv[3];
  const int ne = hasU ? (c_u + 1) >> 1 : 0;
  const unsigned hq = pin(WS_H + ((t0 >> 3) + (wq >> 1)) * 18432 + kh * 9216 + (wq & 1) * 4096);
  const unsigned lq = pin(kh * L_G1 + (wq >> 1) * HB + (wq & 1) * 4096);
  const unsigned og = pin(WS_O + (t0 >> 4) * 18432), wfo = pin(WS_WF + (4 * ct3 + 2 * (wq & 1)) * 18432 + (wq >> 1) * 9216 + kh * 4096);

  const unsigned sha = L_S + l16, shb = L_P + 3584 + lane * 8;
  u32x4 qg;
  auto pre1 = [&]() {
    unsigned ln = lane;
    asm volatile("" : "+v"(ln));
    ln &= 63;
    const unsigned c0 = 18 * head + 17 * (ln >> 5), wb = 3 * ct3 + min(ln >> 3, 2u);
    fo[0] = fa(576 + c0, ln & 31); fo[1] = fa(1152 + c0, ln & 31); fo[2] = fa(c0, 4 * qb + wq);
    qg = tile_addr(ln, QS, 2 * QS);
    asm volatile("v_mov_b32 v245, %5" : "+v"(fo[0]), "+v"(fo[1]), "+v"(fo[2]), "+v"(qg), "+{v[244:251]}"(ta)
                 : "v"(WS_FA + fslot(wb & 7, 28 + (wb >> 3), min(ln & 7, 5u))));
  };
  if (hasU) {
    const int ln = lane;
    const int blk = (ln >> 4) & 1, q = (ln >> 2) & 3, p = ln & 3, d = ln & 31, g4 = ln >> 4, r16 = ln & 15;
    sp[14] = lpa + kh * 3584;
    sp[15] = lpb + kh * 3584;
    sp[22] = (w * 8 + (ln >> 3)) * TS + (ln & 7) * 4;
    sp[23] = (ln & 4) ? 0u : 0x3f803f80u;
    sp[27] = L_E16 + kh * TS + seg * 256 - 256 + d * 8;
    const unsigned qa = L_Q + wq * TS + (2 * blk + (p >> 1)) * 288 + q * 16 + (p & 1) * 8;
    sp[29] = qa + h * 128 + (1 - kh) * 1152;
    sp[30] = qa + h * 128 + kh * 1152;
    sp[31] = qa + (h ^ kh ^ 1) * 1152 + 256;
    const unsigned pk = (2 * blk + (p >> 1)) * 576 + (p & 1) * 8;
    const unsigned aK = 32 + pk + (8 * h + q) * 16;
    const unsigned aX = q < 2 ? 32 + pk + (16 + q) * 16 : (q - 2) * 16;
    const unsigned aV0 = d < 18 ? 32 + h * 576 + 288 + d * 16 : (d == 18 ? 0 : 16);
    const unsigned aV1 = d < 18 ? aV0 + 1152 : aV0;
    mi = u32x8{0xdddd, aK + kh * L_G1, aX + kh * L_G1, aV0 + kh * L_G1, aV1 + kh * L_G1, ln * 16u, ln * 4u, aK};
    asm volatile("" : "+{v[208:239]}"(sp), "+{v[128:135]}"(mi) : "s"(P.xa), "s"(P.wqkv));
    asm volatile("s_load_dwordx2 %0, %1, 0x0" : "={s[46:47]}"(tag) : "s"(ws + WS_EP) : "memory");
    if (__builtin_expect(!isA, 0)) {
      issue_x(1);
      if (hasW) {
        const int half = wst >= 9, step = wst - 9 * half, e = lane >> 3;
        const unsigned v = (step < 8 ? (e & 3) + 8 * (e >> 2) : (e & 1) + 72 * (e >> 1)) * 1152 + (lane & 7) * 16;
        const unsigned s1 = (step < 8 ? 4 : 18) * 1152 - 1024, s2 = (step < 8 ? 18 : 36) * 1152 - 2048;
        dma4(P.wo + (288 * half + (step < 8 ? 36 * step : 16)) * 1152 + wc3 * 128, L_W + w * 4096, v, v + s1, v + s2, v + s1 + s2);
      }
    }
    const unsigned ob = WS_O + (2 * t32 + blk) * 18432;
    ta[0] = (64 * ct3 + 32 * (wq & 1) + (ln >> 2)) * 1152 + (wq >> 1) * 576 + ((p ^ ((ln >> 3) & 3)) << 4);
    ta[2] = L_TOP + kh * 8192 + wq * 1536 + ln * 8;
    ta[3] = ob + (head >> 1) * 1024 + ((head & 1) * 2 + h) * 256 + r16 * 16;
    ta[4] = ob + 16384 + head * 64 + q * 16;
    ta[5] = kh * L_G1 + (wq >> 1) * 4608 + (p >> 1) * HB + (8 * g4 + q) * 16 + (p & 1) * 8;
    ta[7] = g4 * 1152 + r16 * 8;
    tb[0] = r16 * 8;
    tb[2] = kh * L_G1 + wq * 4096 + r16 * 64 + ((g4 ^ ((r16 >> 1) & 3)) << 4);
    tb[3] = ta[0] + 17408;
    wf1 = tile_addr(ln, 9216, QS);
    asm volatile("" : "+{v[244:251]}"(ta), "+{v[0:7]}"(tb), "+{v[240:243]}"(wf1));
    const int wr = wq >> 1, wc = wq & 1;
    u32x8 ab;
    const unsigned ar = 64 * wr + perm32(lane & 31), xr = 32 * wq + perm32(lane & 31), br = lane & 31, sb = isA ? 131072 : 98304;
    const unsigned xb = sb + xr * 128 + ((h ^ ((xr >> 1) & 7)) << 4), bb = sb + 16384 + kh * 8192 + br * 128 + ((h ^ ((br >> 1) & 7)) << 4);
#pragma unroll
    for (int k = 0; k < 4; ++k) { ab[k] = xb ^ (32 * k); ab[4 + k] = bb ^ (32 * k); }
    if (__builtin_expect(isA, 1)) {
      asm volatile(
          "s_cmp_eq_u32 %7, 1\n s_cbranch_scc1 1f\n s_cmp_eq_u32 %7, 2\n s_cbranch_scc1 2f\n s_cmp_eq_u32 %7, 3\n s_cbranch_scc1 3f\n"
          "ds_write_b128 %5, v[128:131]\n ds_write_b128 %5, v[132:135] offset:1024\n s_branch 4f\n"
          "1: ds_write_b128 %5, v[244:247] offset:2048\n ds_write_b128 %5, v[248:251] offset:3072\n s_branch 4f\n"
          "2: ds_write_b128 %5, v[0:3] offset:4096\n ds_write_b128 %5, v[240:243] offset:6144\n s_branch 4f\n"
          "3: ds_write_b128 %5, v[236:239] offset:7168\n ds_write_b64 %6, v[230:231]\n 4:"
          :: "{v[208:239]}"(sp), "{v[128:135]}"(mi), "{v[244:251]}"(ta), "{v[0:7]}"(tb), "{v[240:243]}"(wf1), "v"(sha), "v"(shb), "s"(wq)
          : "memory", "scc");
      const unsigned xa = 32768 + ar * 128 + ((h ^ ((ar >> 1) & 7)) << 4);
#pragma unroll
      for (int k = 0; k < 4; ++k) aa[k] = xa ^ (32 * k);
      aa[4] = 49152 + (8 * h + q) * 256 + (((8 * wc + 2 * blk + (p >> 1)) ^ (q << 2)) << 4) + (p & 1) * 8;
      aa[5] = aa[4] ^ 64;
      aa[6] = (16 * rt + 8 * wr + 2 * h) * QS + (128 * ct + 64 * wc + br) * 16;
    } else {
      issue_x(2);
      if (kh) pre1();
      wait_vm<8>();
      const Cx lam = ssm_part1(raw);
      ssm_part3(ssm_part2(raw, lam, sp[14]), seg, sp[14], sp[15]);
      wait_vm<4>();
      if (hasW) {
        const unsigned ra = L_W + w * 4096 + (8 * g4 + q) * 128 + p * 8;
#pragma unroll
        for (int k = 0; k < 4; ++k) st16(ws + WS_WF + (4 * wc3 + k) * 18432 + wst * 1024, l16, cat(lds_tr(ra + k * 32), lds_tr(ra + k * 32 + 512)));
      }
    }
    const int c64 = 2 * ct + wc, em = c64 < 9 ? 15 : c64 < 27 ? 3 : 1;
    u64 dl = isA ? 0xf7fd10f60303f7fdull : 0x040404f4040404f4ull;
    unsigned cnt, tmp;
    asm volatile(
    "s_cmp_eq_u32 %[em], 0\n"
    "s_cbranch_scc1 9f\n"
    "s_mov_b32 %[cnt], 0\n"
    "s_mov_b32 s50, 0xffff0000\n"
    "s_mov_b32 s51, 0x3eae1a92\n"
    "s_barrier\n"
    "ds_read_b128 v[72:75], v104 offset:0\n"
    "ds_read_b64_tr_b16 v[80:81], v108 offset:0\n"
    "ds_read_b64_tr_b16 v[82:83], v108 offset:1024\n"
    "ds_read_b128 v[76:79], v104 offset:4096\n"
    "ds_read_b64_tr_b16 v[84:85], v109 offset:0\n"
    "ds_read_b64_tr_b16 v[86:87], v109 offset:1024\n"
    "s_waitcnt lgkmcnt(3)\n"
    "v_mfma_f32_32x32x16_bf16 v[8:23], v[72:75], v[80:83], 0\n"
    "ds_read_b128 v[88:91], v105 offset:0\n"
    "ds_read_b64_tr_b16 v[96:97], v108 offset:4096\n"
    "ds_read_b64_tr_b16 v[98:99], v108 offset:5120\n"
    "s_waitcnt lgkmcnt(5)\n"
    "v_mfma_f32_32x32x16_bf16 v[24:39], v[76:79], v[80:83], 0\n"
    "ds_read_b128 v[92:95], v105 offset:4096\n"
    "s_waitcnt lgkmcnt(4)\n"
    "v_mfma_f32_32x32x16_bf16 v[40:55], v[72:75], v[84:87], 0\n"
    "ds_read_b64_tr_b16 v[100:101], v109 offset:4096\n"
    "ds_read_b64_tr_b16 v[102:103], v109 offset:5120\n"
    "v_mfma_f32_32x32x16_bf16 v[56:71], v[76:79], v[84:87], 0\n"
    "s_branch 3f\n"
    "1:\n"
    "s_waitcnt lgkmcnt(3)\n"
    "v_mfma_f32_32x32x16_bf16 v[8:23], v[72:75], v[80:83], v[8:23]\n"
    "ds_read_b128 v[88:91], v105 offset:0\n"
    "ds_read_b64_tr_b16 v[96:97], v108 offset:4096\n"
    "ds_read_b64_tr_b16 v[98:99], v108 offset:5120\n"
    "s_waitcnt lgkmcnt(5)\n"
    "v_mfma_f32_32x32x16_bf16 v[24:39], v[76:79], v[80:83], v[24:39]\n"
    "ds_read_b128 v[92:95], v105 offset:4096\n"
    "s_waitcnt lgkmcnt(4)\n"
    "v_mfma_f32_32x32x16_bf16 v[40:55], v[72:75], v[84:87], v[40:55]\n"
    "ds_read_b64_tr_b16 v[100:101], v109 offset:4096\n"
    "ds_read_b64_tr_b16 v[102:103], v109 offset:5120\n"
    "v_mfma_f32_32x32x16_bf16 v[56:71], v[76:79], v[84:87], v[56:71]\n"
    "3:\n"
    "s_add_u32 %[cnt], %[cnt], 1\n"
    "s_bitcmp0_b32 0x124, %[cnt]\n"
    "s_cselect_b32 %[tmp], 0x8000, s50\n"
    "s_waitcnt lgkmcnt(3)\n"
    "v_mfma_f32_32x32x16_bf16 v[8:23], v[88:91], v[96:99], v[8:23]\n"
    "ds_read_b128 v[72:75], v106 offset:0\n"
    "ds_read_b64_tr_b16 v[80:81], v108 offset:8192\n"
    "ds_read_b64_tr_b16 v[82:83], v108 offset:9216\n"
    "s_waitcnt lgkmcnt(5)\n"
    "v_mfma_f32_32x32x16_bf16 v[24:39], v[92:95], v[96:99], v[24:39]\n"
    "ds_read_b128 v[76:79], v106 offset:4096\n"
    "s_waitcnt lgkmcnt(4)\n"
    "v_mfma_f32_32x32x16_bf16 v[40:55], v[88:91], v[100:103], v[40:55]\n"
    "ds_read_b64_tr_b16 v[84:85], v109 offset:8192\n"
    "ds_read_b64_tr_b16 v[86:87], v109 offset:9216\n"
    "v_mfma_f32_32x32x16_bf16 v[56:71], v[92:95], v[100:103], v[56:71]\n"
    "s_waitcnt lgkmcnt(3)\n"
    "v_mfma_f32_32x32x16_bf16 v[8:23], v[72:75], v[80:83], v[8:23]\n"
    "ds_read_b128 v[88:91], v107 offset:0\n"
    "ds_read_b64_tr_b16 v[96:97], v108 offset:12288\n"
    "ds_read_b64_tr_b16 v[98:99], v108 offset:13312\n"
    "s_waitcnt lgkmcnt(5)\n"
    "v_mfma_f32_32x32x16_bf16 v[24:39], v[76:79], v[80:83], v[24:39]\n"
    "ds_read_b128 v[92:95], v107 offset:4096\n"
    "s_waitcnt lgkmcnt(4)\n"
    "v_mfma_f32_32x32x16_bf16 v[40:55], v[72:75], v[84:87], v[40:55]\n"
    "ds_read_b64_tr_b16 v[100:101], v109 offset:12288\n"
    "ds_read_b64_tr_b16 v[102:103], v109 offset:13312\n"
    "v_mfma_f32_32x32x16_bf16 v[56:71], v[76:79], v[84:87], v[56:71]\n"
    "s_waitcnt lgkmcnt(3)\n"
    "v_mfma_f32_32x32x16_bf16 v[8:23], v[88:91], v[96:99], v[8:23]\n"
    "v_add_u32 v104, %[tmp], v104\n"
    "v_add_u32 v105, %[tmp], v105\n"
    "v_add_u32 v106, %[tmp], v106\n"
    "v_add_u32 v107, %[tmp], v107\n"
    "v_add_u32 v108, %[tmp], v108\n"
    "v_add_u32 v109, %[tmp], v109\n"
    "s_cmp_eq_u32 %[cnt], 9\n"
    "s_waitcnt lgkmcnt(0)\n"
    "v_mfma_f32_32x32x16_bf16 v[24:39], v[92:95], v[96:99], v[24:39]\n"
    "s_cbranch_scc1 2f\n"
    "s_barrier\n"
    "ds_read_b128 v[72:75], v104 offset:0\n"
    "ds_read_b64_tr_b16 v[80:81], v108 offset:0\n"
    "ds_read_b64_tr_b16 v[82:83], v108 offset:1024\n"
    "ds_read_b128 v[76:79], v104 offset:4096\n"
    "v_mfma_f32_32x32x16_bf16 v[40:55], v[88:91], v[100:103], v[40:55]\n"
    "ds_read_b64_tr_b16 v[84:85], v109 offset:0\n"
    "ds_read_b64_tr_b16 v[86:87], v109 offset:1024\n"
    "v_mfma_f32_32x32x16_bf16 v[56:71], v[92:95], v[100:103], v[56:71]\n"
    "s_branch 1b\n"
    "2:\n"
    "v_mfma_f32_32x32x16_bf16 v[40:55], v[88:91], v[100:103], v[40:55]\n"
    "v_mfma_f32_32x32x16_bf16 v[56:71], v[92:95], v[100:103], v[56:71]\n"
    "s_setprio 2\n"
    "s_bitcmp1_b32 %[em], 1\n"
    "s_cbranch_scc0 8f\n"
    "v_add_u32 v104, 0, v110\n"
    "v_add_u32 v105, 27648, v110\n"
    "v_add_u32 v106, 110592, v110\n"
    "v_add_u32 v107, 138240, v110\n"
    "s_bitcmp1_b32 %[em], 2\n"
    "s_cbranch_scc1 4f\n"
    "v_cvt_pk_bf16_f32 v72, v8, v9\n"
    "v_cvt_pk_bf16_f32 v73, v10, v11\n"
    "v_cvt_pk_bf16_f32 v74, v12, v13\n"
    "v_cvt_pk_bf16_f32 v75, v14, v15\n"
    "global_store_dwordx4 v104, v[72:75], %[ws] offset:0 sc1\n"
    "v_cvt_pk_bf16_f32 v76, v16, v17\n"
    "v_cvt_pk_bf16_f32 v77, v18, v19\n"
    "v_cvt_pk_bf16_f32 v78, v20, v21\n"
    "v_cvt_pk_bf16_f32 v79, v22, v23\n"
    "global_store_dwordx4 v105, v[76:79], %[ws] offset:0 sc1\n"
    "v_cvt_pk_bf16_f32 v80, v24, v25\n"
    "v_cvt_pk_bf16_f32 v81, v26, v27\n"
    "v_cvt_pk_bf16_f32 v82, v28, v29\n"
    "v_cvt_pk_bf16_f32 v83, v30, v31\n"
    "global_store_dwordx4 v106, v[80:83], %[ws] offset:0 sc1\n"
    "v_cvt_pk_bf16_f32 v84, v32, v33\n"
    "v_cvt_pk_bf16_f32 v85, v34, v35\n"
    "v_cvt_pk_bf16_f32 v86, v36, v37\n"
    "v_cvt_pk_bf16_f32 v87, v38, v39\n"
    "global_store_dwordx4 v107, v[84:87], %[ws] offset:0 sc1\n"
    "s_branch 5f\n"
    "4:\n"
    "v_mul_f32 v8, s51, v8\n"
    "v_mul_f32 v9, s51, v9\n"
    "v_mul_f32 v10, s51, v10\n"
    "v_mul_f32 v11, s51, v11\n"
    "v_mul_f32 v12, s51, v12\n"
    "v_mul_f32 v13, s51, v13\n"
    "v_mul_f32 v14, s51, v14\n"
    "v_mul_f32 v15, s51, v15\n"
    "v_cvt_pk_bf16_f32 v72, v8, v9\n"
    "v_cvt_pk_bf16_f32 v73, v10, v11\n"
    "v_cvt_pk_bf16_f32 v74, v12, v13\n"
    "v_cvt_pk_bf16_f32 v75, v14, v15\n"
    "global_store_dwordx4 v104, v[72:75], %[ws] offset:0 sc1\n"
    "v_mul_f32 v16, s51, v16\n"
    "v_mul_f32 v17, s51, v17\n"
    "v_mul_f32 v18, s51, v18\n"
    "v_mul_f32 v19, s51, v19\n"
    "v_mul_f32 v20, s51, v20\n"
    "v_mul_f32 v21, s51, v21\n"
    "v_mul_f32 v22, s51, v22\n"
    "v_mul_f32 v23, s51, v23\n"
    "v_cvt_pk_bf16_f32 v76, v16, v17\n"
    "v_cvt_pk_bf16_f32 v77, v18, v19\n"
    "v_cvt_pk_bf16_f32 v78, v20, v21\n"
    "v_cvt_pk_bf16_f32 v79, v22, v23\n"
    "global_store_dwordx4 v105, v[76:79], %[ws] offset:0 sc1\n"
    "v_mul_f32 v24, s51, v24\n"
    "v_mul_f32 v25, s51, v25\n"
    "v_mul_f32 v26, s51, v26\n"
    "v_mul_f32 v27, s51, v27\n"
    "v_mul_f32 v28, s51, v28\n"
    "v_mul_f32 v29, s51, v29\n"
    "v_mul_f32 v30, s51, v30\n"
    "v_mul_f32 v31, s51, v31\n"
    "v_cvt_pk_bf16_f32 v80, v24, v25\n"
    "v_cvt_pk_bf16_f32 v81, v26, v27\n"
    "v_cvt_pk_bf16_f32 v82, v28, v29\n"
    "v_cvt_pk_bf16_f32 v83, v30, v31\n"
    "global_store_dwordx4 v106, v[80:83], %[ws] offset:0 sc1\n"
    "v_mul_f32 v32, s51, v32\n"
    "v_mul_f32 v33, s51, v33\n"
    "v_mul_f32 v34, s51, v34\n"
    "v_mul_f32 v35, s51, v35\n"
    "v_mul_f32 v36, s51, v36\n"
    "v_mul_f32 v37, s51, v37\n"
    "v_mul_f32 v38, s51, v38\n"
    "v_mul_f32 v39, s51, v39\n"
    "v_cvt_pk_bf16_f32 v84, v32, v33\n"
    "v_cvt_pk_bf16_f32 v85, v34, v35\n"
    "v_cvt_pk_bf16_f32 v86, v36, v37\n"
    "v_cvt_pk_bf16_f32 v87, v38, v39\n"
    "global_store_dwordx4 v107, v[84:87], %[ws] offset:0 sc1\n"
    "5:\n"
    "s_bitcmp1_b32 %[em], 3\n"
    "s_cbranch_scc1 4f\n"
    "v_cvt_pk_bf16_f32 v88, v40, v41\n"
    "v_cvt_pk_bf16_f32 v89, v42, v43\n"
    "v_cvt_pk_bf16_f32 v90, v44, v45\n"
    "v_cvt_pk_bf16_f32 v91, v46, v47\n"
    "global_store_dwordx4 v104, v[88:91], %[ws] offset:512 sc1\n"
    "v_cvt_pk_bf16_f32 v92, v48, v49\n"
    "v_cvt_pk_bf16_f32 v93, v50, v51\n"
    "v_cvt_pk_bf16_f32 v94, v52, v53\n"
    "v_cvt_pk_bf16_f32 v95, v54, v55\n"
    "global_store_dwordx4 v105, v[92:95], %[ws] offset:512 sc1\n"
    "v_cvt_pk_bf16_f32 v96, v56, v57\n"
    "v_cvt_pk_bf16_f32 v97, v58, v59\n"
    "v_cvt_pk_bf16_f32 v98, v60, v61\n"
    "v_cvt_pk_bf16_f32 v99, v62, v63\n"
    "global_store_dwordx4 v106, v[96:99], %[ws] offset:512 sc1\n"
    "v_cvt_pk_bf16_f32 v100, v64, v65\n"
    "v_cvt_pk_bf16_f32 v101, v66, v67\n"
    "v_cvt_pk_bf16_f32 v102, v68, v69\n"
    "v_cvt_pk_bf16_f32 v103, v70, v71\n"
    "global_store_dwordx4 v107, v[100:103], %[ws] offset:512 sc1\n"
    "s_branch 5f\n"
    "4:\n"
    "v_mul_f32 v40, s51, v40\n"
    "v_mul_f32 v41, s51, v41\n"
    "v_mul_f32 v42, s51, v42\n"
    "v_mul_f32 v43, s51, v43\n"
    "v_mul_f32 v44, s51, v44\n"
    "v_mul_f32 v45, s51, v45\n"
    "v_mul_f32 v46, s51, v46\n"
    "v_mul_f32 v47, s51, v47\n"
    "v_cvt_pk_bf16_f32 v88, v40, v41\n"
    "v_cvt_pk_bf16_f32 v89, v42, v43\n"
    "v_cvt_pk_bf16_f32 v90, v44, v45\n"
    "v_cvt_pk_bf16_f32 v91, v46, v47\n"
    "global_store_dwordx4 v104, v[88:91], %[ws] offset:512 sc1\n"
    "v_mul_f32 v48, s51, v48\n"
    "v_mul_f32 v49, s51, v49\n"
    "v_mul_f32 v50, s51, v50\n"
    "v_mul_f32 v51, s51, v51\n"
    "v_mul_f32 v52, s51, v52\n"
    "v_mul_f32 v53, s51, v53\n"
    "v_mul_f32 v54, s51, v54\n"
    "v_mul_f32 v55, s51, v55\n"
    "v_cvt_pk_bf16_f32 v92, v48, v49\n"
    "v_cvt_pk_bf16_f32 v93, v50, v51\n"
    "v_cvt_pk_bf16_f32 v94, v52, v53\n"
    "v_cvt_pk_bf16_f32 v95, v54, v55\n"
    "global_store_dwordx4 v105, v[92:95], %[ws] offset:512 sc1\n"
    "v_mul_f32 v56, s51, v56\n"
    "v_mul_f32 v57, s51, v57\n"
    "v_mul_f32 v58, s51, v58\n"
    "v_mul_f32 v59, s51, v59\n"
    "v_mul_f32 v60, s51, v60\n"
    "v_mul_f32 v61, s51, v61\n"
    "v_mul_f32 v62, s51, v62\n"
    "v_mul_f32 v63, s51, v63\n"
    "v_cvt_pk_bf16_f32 v96, v56, v57\n"
    "v_cvt_pk_bf16_f32 v97, v58, v59\n"
    "v_cvt_pk_bf16_f32 v98, v60, v61\n"
    "v_cvt_pk_bf16_f32 v99, v62, v63\n"
    "global_store_dwordx4 v106, v[96:99], %[ws] offset:512 sc1\n"
    "v_mul_f32 v64, s51, v64\n"
    "v_mul_f32 v65, s51, v65\n"
    "v_mul_f32 v66, s51, v66\n"
    "v_mul_f32 v67, s51, v67\n"
    "v_mul_f32 v68, s51, v68\n"
    "v_mul_f32 v69, s51, v69\n"
    "v_mul_f32 v70, s51, v70\n"
    "v_mul_f32 v71, s51, v71\n"
    "v_cvt_pk_bf16_f32 v100, v64, v65\n"
    "v_cvt_pk_bf16_f32 v101, v66, v67\n"
    "v_cvt_pk_bf16_f32 v102, v68, v69\n"
    "v_cvt_pk_bf16_f32 v103, v70, v71\n"
    "global_store_dwordx4 v107, v[100:103], %[ws] offset:512 sc1\n"
    "5:\n"
    "s_branch 9f\n"
    "8:\n"
    "s_nop 10\n"
    "9:\n"
    "s_waitcnt lgkmcnt(0)\n"
    "s_add_u32 s46, s46, 1\n"
    "s_addc_u32 s47, s47, 0\n"
    "s_mov_b32 %[cnt], 0\n"
    "s_mov_b32 s50, 0x8000\n"
    "s_barrier\n"
    "ds_read_b128 v[8:11], v168 offset:0\n"
    "ds_read_b128 v[12:15], v172 offset:0\n"
    "ds_read_b128 v[16:19], v172 offset:4096\n"
    "ds_read_b128 v[20:23], v169 offset:0\n"
    "ds_read_b128 v[24:27], v173 offset:0\n"
    "ds_read_b128 v[28:31], v173 offset:4096\n"
    "s_waitcnt lgkmcnt(4)\n"
    "v_mfma_f32_32x32x16_bf16 v[136:151], v[8:11], v[12:15], 0\n"
    "ds_read_b128 v[32:35], v170 offset:0\n"
    "ds_read_b128 v[36:39], v174 offset:0\n"
    "s_waitcnt lgkmcnt(5)\n"
    "v_mfma_f32_32x32x16_bf16 v[152:167], v[8:11], v[16:19], 0\n"
    "ds_read_b128 v[40:43], v174 offset:4096\n"
    "s_branch 4f\n"
    "1:\n"
    "s_waitcnt lgkmcnt(4)\n"
    "v_mfma_f32_32x32x16_bf16 v[136:151], v[8:11], v[12:15], v[136:151]\n"
    "ds_read_b128 v[32:35], v170 offset:0\n"
    "ds_read_b128 v[36:39], v174 offset:0\n"
    "s_waitcnt lgkmcnt(5)\n"
    "v_mfma_f32_32x32x16_bf16 v[152:167], v[8:11], v[16:19], v[152:167]\n"
    "ds_read_b128 v[40:43], v174 offset:4096\n"
    "4:\n"
    "s_sext_i32_i8 %[tmp], s48\n"
    "s_lshl_b32 %[tmp], %[tmp], 13\n"
    "s_lshr_b64 s[48:49], s[48:49], 8\n"
    "s_cmp_ge_u32 %[cnt], %[xl]\n"
    "s_cbranch_scc1 7f\n"
    "s_add_u32 s50, s50, 0x8000\n"
    "s_and_b32 s50, s50, 0x18000\n"
    "s_add_u32 m0, s50, %[lx]\n"
    "v_add_u32 v104, 0x80, v104\n"
    "v_add_u32 v105, 0x80, v105\n"
    "v_add_u32 v106, 0x80, v106\n"
    "global_load_lds_dwordx4 v104, %[xg]\n"
    "global_load_lds_dwordx4 v105, %[xg] offset:1024\n"
    "s_add_u32 m0, s50, %[lb]\n"
    "s_nop 0\n"
    "global_load_lds_dwordx4 v106, %[rg]\n"
    "s_add_u32 m0, m0, 0x1000\n"
    "s_nop 0\n"
    "global_load_lds_dwordx4 v106, %[ig]\n"
    "7:\n"
    "s_waitcnt lgkmcnt(4)\n"
    "v_mfma_f32_32x32x16_bf16 v[136:151], v[20:23], v[24:27], v[136:151]\n"
    "ds_read_b128 v[44:47], v171 offset:0\n"
    "ds_read_b128 v[48:51], v175 offset:0\n"
    "s_waitcnt lgkmcnt(5)\n"
    "v_mfma_f32_32x32x16_bf16 v[152:167], v[20:23], v[28:31], v[152:167]\n"
    "ds_read_b128 v[52:55], v175 offset:4096\n"
    "s_waitcnt lgkmcnt(4)\n"
    "v_mfma_f32_32x32x16_bf16 v[136:151], v[32:35], v[36:39], v[136:151]\n"
    "v_add_u32 v168, %[tmp], v168\n"
    "v_add_u32 v169, %[tmp], v169\n"
    "v_add_u32 v170, %[tmp], v170\n"
    "v_add_u32 v171, %[tmp], v171\n"
    "v_add_u32 v172, %[tmp], v172\n"
    "v_add_u32 v173, %[tmp], v173\n"
    "v_add_u32 v174, %[tmp], v174\n"
    "v_add_u32 v175, %[tmp], v175\n"
    "s_cmp_lg_u32 %[cnt], %[fs]\n"
    "s_cbranch_scc1 3f\n"
    "v_mov_b32 v252, s46\n"
    "v_mov_b32 v253, s47\n"
    "v_mov_b32 v254, %[fa]\n"
    "s_waitcnt vmcnt(0)\n"
    "s_mov_b64 exec, 1\n"
    "global_store_dwordx2 v254, v[252:253], %[ws] sc1\n"
    "s_mov_b64 exec, -1\n"
    "3:\n"
    "s_cmp_lg_u32 %[cnt], 2\n"
    "s_cbranch_scc1 5f\n"
    "v_mbcnt_lo_u32_b32 v56, -1, 0\n"
    "v_mbcnt_hi_u32_b32 v56, -1, v56\n"
    "s_bfe_u32 s52, %[pk], 0x80008\n"
    "s_and_b32 s53, %[pk], 0xff\n"
    "v_lshrrev_b32 v57, 5, v56\n"
    "v_and_b32 v58, 31, v56\n"
    "s_mul_i32 s54, s52, 17\n"
    "s_add_u32 s54, s54, s53\n"
    "s_lshl_b32 s54, s54, 9\n"
    "s_add_u32 s54, s54, 0xe22000\n"
    "v_lshl_add_u32 v59, v58, 4, s54\n"
    "v_lshlrev_b32 v60, 9, v57\n"
    "v_add_u32 v233, v59, v60\n"
    "v_sub_u32 v232, v59, v60\n"
    "v_subrev_u32 v232, 3072, v232\n"
    "s_bfe_u32 s55, %[pk], 0x20010\n"
    "s_lshl2_add_u32 s53, s53, s55\n"
    "s_mul_i32 s53, s53, 73728\n"
    "s_lshl_b32 s52, s52, 9\n"
    "s_add_u32 s53, s53, s52\n"
    "s_add_u32 s53, s53, 0x900000\n"
    "v_mul_u32_u24 v60, 36864, v57\n"
    "v_lshl_add_u32 v59, v58, 4, s53\n"
    "v_add_u32 v234, v59, v60\n"
    "s_movk_i32 s52, 2336\n"
    "s_bitcmp1_b32 %[pk], 18\n"
    "s_cselect_b32 s52, s52, 0\n"
    "v_lshl_add_u32 v59, v58, 3, s52\n"
    "v_add_u32 v236, 67776, v59\n"
    "s_cbranch_scc1 5f\n"
    "s_lshr_b32 s52, %[pk], 25\n"
    "s_lshr_b32 s53, %[pk], 24\n"
    "s_add_u32 s53, s53, 4\n"
    "s_lshr_b32 s53, s53, 1\n"
    "s_add_u32 s54, s52, s55\n"
    "s_min_u32 s54, s54, s53\n"
    "s_and_b32 s55, s55, 1\n"
    "s_lshl1_add_u32 s52, s55, s52\n"
    "v_add_u32 v59, s52, v57\n"
    "v_min_u32 v59, s53, v59\n"
    "s_lshl_b32 s52, s54, 8\n"
    "s_or_b32 s52, s52, s54\n"
    "s_and_b32 s52, s52, 0xc70\n"
    "s_and_b32 s54, s54, 3\n"
    "s_lshl_b32 s54, s54, 7\n"
    "s_add_u32 s52, s52, s54\n"
    "s_add_u32 s52, s52, 0xe50400\n"
    "v_lshlrev_b32 v60, 12, v58\n"
    "v_min_u32 v60, 29184, v60\n"
    "v_lshl_add_u32 v60, v57, 3, v60\n"
    "v_add_u32 v245, s52, v60\n"
    "v_lshrrev_b32 v60, 3, v59\n"
    "v_and_or_b32 v60, v58, 24, v60\n"
    "v_and_b32 v57, 1, v59\n"
    "v_lshl_add_u32 v60, v60, 1, v57\n"
    "v_bfe_u32 v59, v59, 1, 2\n"
    "v_lshl_add_u32 v60, v60, 2, v59\n"
    "v_and_b32 v58, 7, v58\n"
    "v_lshl_add_u32 v60, v60, 4, v58\n"
    "v_lshlrev_b32 v60, 3, v60\n"
    "v_add_u32 v250, 0xe58400, v60\n"
    "5:\n"
    "s_add_u32 %[cnt], %[cnt], 1\n"
    "s_cmp_eq_u32 %[cnt], 9\n"
    "s_waitcnt lgkmcnt(0)\n"
    "v_mfma_f32_32x32x16_bf16 v[152:167], v[32:35], v[40:43], v[152:167]\n"
    "s_cbranch_scc1 2f\n"
    "s_cmp_lg_u32 %[em], 0\n"
    "s_cbranch_scc1 6f\n"
    "s_waitcnt vmcnt(8)\n"
    "s_cmp_lt_u32 %[cnt], 7\n"
    "s_cbranch_scc1 6f\n"
    "s_waitcnt vmcnt(4)\n"
    "s_cmp_lt_u32 %[cnt], 8\n"
    "s_cbranch_scc1 6f\n"
    "s_waitcnt vmcnt(0)\n"
    "6:\n"
    "s_barrier\n"
    "ds_read_b128 v[8:11], v168 offset:0\n"
    "ds_read_b128 v[12:15], v172 offset:0\n"
    "ds_read_b128 v[16:19], v172 offset:4096\n"
    "v_mfma_f32_32x32x16_bf16 v[136:151], v[44:47], v[48:51], v[136:151]\n"
    "ds_read_b128 v[20:23], v169 offset:0\n"
    "ds_read_b128 v[24:27], v173 offset:0\n"
    "ds_read_b128 v[28:31], v173 offset:4096\n"
    "v_mfma_f32_32x32x16_bf16 v[152:167], v[44:47], v[52:55], v[152:167]\n"
    "s_branch 1b\n"
    "2:\n"
    "v_mfma_f32_32x32x16_bf16 v[136:151], v[44:47], v[48:51], v[136:151]\n"
    "ds_read_b128 v[208:211], v222 offset:0\n"
    "ds_read_b128 v[212:215], v222 offset:16\n"
    "ds_read_b128 v[216:219], v222 offset:32\n"
    "ds_read_b64 v[220:221], v223\n"
    "v_mfma_f32_32x32x16_bf16 v[152:167], v[44:47], v[52:55], v[152:167]\n"
    "s_setprio 0\n"
    "s_nop 10\n"
        : "={v[136:167]}"(uu), "+{v[104:111]}"(aa), "+{v[168:175]}"(ab), "+{s[48:49]}"(dl), "+{s[46:47]}"(tag),
          "+{v[208:239]}"(sp), "+{v[244:251]}"(ta), [cnt] "=&s"(cnt), [tmp] "=&s"(tmp)
        : [em] "s"(isA ? em : 0), [fs] "s"(isA ? 2 : -1), [fa] "s"(WS_FA + myf), [ws] "s"(ws), [xl] "s"(isA ? 0 : 6),
          [lx] "s"(lx), [lb] "s"(lb), [xg] "s"(P.xb + 256), [rg] "s"(P.bre + 256), [ig] "s"(P.bim + 256),
          [pk] "s"(c_u | pg_u << 8 | w << 16 | t0 << 20)
        : "memory", "scc", "v8", "v9", "v10", "v11", "v12", "v13", "v14", "v15", "v16", "v17", "v18", "v19", "v20", "v21", "v22", "v23", "v24", "v25", "v26", "v27", "v28", "v29", "v30", "v31", "v32", "v33", "v34", "v35", "v36", "v37", "v38", "v39", "v40", "v41", "v42", "v43", "v44", "v45", "v46", "v47", "v48", "v49", "v50", "v51", "v52", "v53", "v54", "v55", "v56", "v57", "v58", "v59", "v60", "v61", "v62", "v63", "v64", "v65", "v66", "v67", "v68", "v69", "v70", "v71", "v72", "v73", "v74", "v75", "v76", "v77", "v78", "v79", "v80", "v81", "v82", "v83", "v84", "v85", "v86", "v87", "v88", "v89", "v90", "v91", "v92", "v93", "v94", "v95", "v96", "v97", "v98", "v99", "v100", "v101", "v102", "v103", "v252", "v253", "v254", "s50", "s51", "s52", "s53", "s54", "s55");
    if (!isA && lane == 0) st8(ws, WS_FA + myf, tag);
    asm volatile(
    "s_waitcnt lgkmcnt(0)\n"
    "v_mul_f32 v8, v208, v208\n"
    "v_mul_f32 v9, v208, v209\n"
    "v_fmac_f32 v8, v210, v209\n"
    "v_add_f32 v9, v9, v9\n"
    "v_pk_mul_f32 v[228:229], v[212:213], v[136:137] op_sel:[1,0] op_sel_hi:[1,1]\n"
    "v_pk_mul_f32 v[136:137], v[210:211], v[136:137] op_sel:[1,0] op_sel_hi:[1,1]\n"
    "v_pk_fma_f32 v[136:137], v[212:213], v[152:153], v[136:137] op_sel:[1,0,0] op_sel_hi:[1,1,1]\n"
    "v_pk_fma_f32 v[152:153], v[212:213], v[152:153], v[228:229] op_sel:[0,0,0] op_sel_hi:[0,1,1]\n"
    "v_pk_mul_f32 v[228:229], v[212:213], v[138:139] op_sel:[1,0] op_sel_hi:[1,1]\n"
    "v_pk_mul_f32 v[138:139], v[210:211], v[138:139] op_sel:[1,0] op_sel_hi:[1,1]\n"
    "v_pk_fma_f32 v[138:139], v[212:213], v[154:155], v[138:139] op_sel:[1,0,0] op_sel_hi:[1,1,1]\n"
    "v_pk_fma_f32 v[154:155], v[212:213], v[154:155], v[228:229] op_sel:[0,0,0] op_sel_hi:[0,1,1]\n"
    "v_pk_fma_f32 v[14:15], v[8:9], v[136:137], v[138:139] op_sel:[0,0,0] op_sel_hi:[0,1,1]\n"
    "v_pk_mul_f32 v[228:229], v[212:213], v[140:141] op_sel:[1,0] op_sel_hi:[1,1]\n"
    "v_pk_fma_f32 v[16:17], v[8:9], v[152:153], v[154:155] op_sel:[0,0,0] op_sel_hi:[0,1,1]\n"
    "v_pk_mul_f32 v[140:141], v[210:211], v[140:141] op_sel:[1,0] op_sel_hi:[1,1]\n"
    "v_pk_fma_f32 v[14:15], v[8:9], v[152:153], v[14:15] op_sel:[1,0,0] op_sel_hi:[1,1,1] neg_lo:[1,0,0] neg_hi:[1,0,0]\n"
    "v_pk_fma_f32 v[140:141], v[212:213], v[156:157], v[140:141] op_sel:[1,0,0] op_sel_hi:[1,1,1]\n"
    "v_pk_fma_f32 v[16:17], v[8:9], v[136:137], v[16:17] op_sel:[1,0,0] op_sel_hi:[1,1,1]\n"
    "v_pk_fma_f32 v[156:157], v[212:213], v[156:157], v[228:229] op_sel:[0,0,0] op_sel_hi:[0,1,1]\n"
    "v_pk_fma_f32 v[10:11], v[8:9], v[14:15], v[140:141] op_sel:[0,0,0] op_sel_hi:[0,1,1]\n"
    "v_pk_mul_f32 v[228:229], v[212:213], v[142:143] op_sel:[1,0] op_sel_hi:[1,1]\n"
    "v_pk_fma_f32 v[12:13], v[8:9], v[16:17], v[156:157] op_sel:[0,0,0] op_sel_hi:[0,1,1]\n"
    "v_pk_mul_f32 v[142:143], v[210:211], v[142:143] op_sel:[1,0] op_sel_hi:[1,1]\n"
    "v_pk_fma_f32 v[10:11], v[8:9], v[16:17], v[10:11] op_sel:[1,0,0] op_sel_hi:[1,1,1] neg_lo:[1,0,0] neg_hi:[1,0,0]\n"
    "v_pk_fma_f32 v[142:143], v[212:213], v[158:159], v[142:143] op_sel:[1,0,0] op_sel_hi:[1,1,1]\n"
    "v_pk_fma_f32 v[12:13], v[8:9], v[14:15], v[12:13] op_sel:[1,0,0] op_sel_hi:[1,1,1]\n"
    "v_pk_fma_f32 v[158:159], v[212:213], v[158:159], v[228:229] op_sel:[0,0,0] op_sel_hi:[0,1,1]\n"
    "v_pk_fma_f32 v[14:15], v[8:9], v[10:11], v[142:143] op_sel:[0,0,0] op_sel_hi:[0,1,1]\n"
    "v_pk_mul_f32 v[228:229], v[212:213], v[144:145] op_sel:[1,0] op_sel_hi:[1,1]\n"
    "v_pk_fma_f32 v[16:17], v[8:9], v[12:13], v[158:159] op_sel:[0,0,0] op_sel_hi:[0,1,1]\n"
    "v_pk_mul_f32 v[144:145], v[210:211], v[144:145] op_sel:[1,0] op_sel_hi:[1,1]\n"
    "v_pk_fma_f32 v[14:15], v[8:9], v[12:13], v[14:15] op_sel:[1,0,0] op_sel_hi:[1,1,1] neg_lo:[1,0,0] neg_hi:[1,0,0]\n"
    "v_pk_fma_f32 v[144:145], v[212:213], v[160:161], v[144:145] op_sel:[1,0,0] op_sel_hi:[1,1,1]\n"
    "v_pk_fma_f32 v[16:17], v[8:9], v[10:11], v[16:17] op_sel:[1,0,0] op_sel_hi:[1,1,1]\n"
    "v_pk_fma_f32 v[160:161], v[212:213], v[160:161], v[228:229] op_sel:[0,0,0] op_sel_hi:[0,1,1]\n"
    "v_pk_fma_f32 v[10:11], v[8:9], v[14:15], v[144:145] op_sel:[0,0,0] op_sel_hi:[0,1,1]\n"
    "v_pk_mul_f32 v[228:229], v[212:213], v[146:147] op_sel:[1,0] op_sel_hi:[1,1]\n"
    "v_pk_fma_f32 v[12:13], v[8:9], v[16:17], v[160:161] op_sel:[0,0,0] op_sel_hi:[0,1,1]\n"
    "v_pk_mul_f32 v[146:147], v[210:211], v[146:147] op_sel:[1,0] op_sel_hi:[1,1]\n"
    "v_pk_fma_f32 v[10:11], v[8:9], v[16:17], v[10:11] op_sel:[1,0,0] op_sel_hi:[1,1,1] neg_lo:[1,0,0] neg_hi:[1,0,0]\n"
    "v_pk_fma_f32 v[146:147], v[212:213], v[162:163], v[146:147] op_sel:[1,0,0] op_sel_hi:[1,1,1]\n"
    "v_pk_fma_f32 v[12:13], v[8:9], v[14:15], v[12:13] op_sel:[1,0,0] op_sel_hi:[1,1,1]\n"
    "v_pk_fma_f32 v[162:163], v[212:213], v[162:163], v[228:229] op_sel:[0,0,0] op_sel_hi:[0,1,1]\n"
    "v_pk_fma_f32 v[14:15], v[8:9], v[10:11], v[146:147] op_sel:[0,0,0] op_sel_hi:[0,1,1]\n"
    "v_pk_mul_f32 v[228:229], v[212:213], v[148:149] op_sel:[1,0] op_sel_hi:[1,1]\n"
    "v_pk_fma_f32 v[16:17], v[8:9], v[12:13], v[162:163] op_sel:[0,0,0] op_sel_hi:[0,1,1]\n"
    "v_pk_mul_f32 v[148:149], v[210:211], v[148:149] op_sel:[1,0] op_sel_hi:[1,1]\n"
    "v_pk_fma_f32 v[14:15], v[8:9], v[12:13], v[14:15] op_sel:[1,0,0] op_sel_hi:[1,1,1] neg_lo:[1,0,0] neg_hi:[1,0,0]\n"
    "v_pk_fma_f32 v[148:149], v[212:213], v[164:165], v[148:149] op_sel:[1,0,0] op_sel_hi:[1,1,1]\n"
    "v_pk_fma_f32 v[16:17], v[8:9], v[10:11], v[16:17] op_sel:[1,0,0] op_sel_hi:[1,1,1]\n"
    "v_pk_fma_f32 v[164:165], v[212:213], v[164:165], v[228:229] op_sel:[0,0,0] op_sel_hi:[0,1,1]\n"
    "v_pk_fma_f32 v[10:11], v[8:9], v[14:15], v[148:149] op_sel:[0,0,0] op_sel_hi:[0,1,1]\n"
    "v_pk_mul_f32 v[228:229], v[212:213], v[150:151] op_sel:[1,0] op_sel_hi:[1,1]\n"
    "v_pk_fma_f32 v[12:13], v[8:9], v[16:17], v[164:165] op_sel:[0,0,0] op_sel_hi:[0,1,1]\n"
    "v_pk_mul_f32 v[150:151], v[210:211], v[150:151] op_sel:[1,0] op_sel_hi:[1,1]\n"
    "v_pk_fma_f32 v[10:11], v[8:9], v[16:17], v[10:11] op_sel:[1,0,0] op_sel_hi:[1,1,1] neg_lo:[1,0,0] neg_hi:[1,0,0]\n"
    "v_pk_fma_f32 v[150:151], v[212:213], v[166:167], v[150:151] op_sel:[1,0,0] op_sel_hi:[1,1,1]\n"
    "v_pk_fma_f32 v[12:13], v[8:9], v[14:15], v[12:13] op_sel:[1,0,0] op_sel_hi:[1,1,1]\n"
    "v_pk_fma_f32 v[166:167], v[212:213], v[166:167], v[228:229] op_sel:[0,0,0] op_sel_hi:[0,1,1]\n"
    "v_pk_fma_f32 v[14:15], v[8:9], v[10:11], v[150:151] op_sel:[0,0,0] op_sel_hi:[0,1,1]\n"
    "v_pk_fma_f32 v[16:17], v[8:9], v[12:13], v[166:167] op_sel:[0,0,0] op_sel_hi:[0,1,1]\n"
    "v_pk_fma_f32 v[14:15], v[8:9], v[12:13], v[14:15] op_sel:[1,0,0] op_sel_hi:[1,1,1] neg_lo:[1,0,0] neg_hi:[1,0,0]\n"
    "v_pk_fma_f32 v[16:17], v[8:9], v[10:11], v[16:17] op_sel:[1,0,0] op_sel_hi:[1,1,1]\n"
    "v_fma_f32 v226, v208, v14, v15\n"
    "v_fma_f32 v227, v208, v16, v17\n"
    "v_fmac_f32 v226, v210, v16\n"
    "v_fmac_f32 v227, v209, v14\n"
    "ds_write_b64 v235, v[226:227] offset:256\n"
        : "+{v[136:167]}"(uu), "+{v[208:239]}"(sp) :: "memory", "v8", "v9", "v10", "v11", "v12", "v13", "v14", "v15", "v16", "v17");
  } else {
    L.b = swz(32 * pg_m + 8 * wq + (lane >> 3));
    asm volatile("s_load_dwordx2 %0, %2, 0x0" : "={s[46:47]}"(tag), "+v"(L.b) : "s"(ws + WS_EP) : "memory");
    int slot = 0;
#pragma nounroll
    for (int s = 2; s < 9; ++s) {
      wait_vm<8>();
      bar();
      issue_a(L, P.xa, P.wqkv, s, slot, wq);
      slot = slot == 2 ? 0 : slot + 1;
    }
    asm volatile("s_add_u32 s46, s46, 1\n s_addc_u32 s47, s47, 0" : "+{s[46:47]}"(tag) :: "scc");
    asm volatile(
        "ds_read_b128 v[128:131], %5\n ds_read_b128 v[132:135], %5 offset:1024\n ds_read_b128 v[244:247], %5 offset:2048\n"
        "ds_read_b128 v[248:251], %5 offset:3072\n ds_read_b128 v[0:3], %5 offset:4096\n"
        "ds_read_b128 v[240:243], %5 offset:6144\n ds_read_b128 v[236:239], %5 offset:7168\n ds_read_b64 v[230:231], %6\n s_waitcnt lgkmcnt(0)\n"
        "v_swap_b32 v237, v238\n v_add_u32 v129, %8, v129\n v_add_u32 v130, %8, v130\n v_add_u32 v131, %8, v131\n v_add_u32 v132, %8, v132\n"
        "v_add_u32 v244, %9, v244\n v_add_u32 v246, %10, v246\n v_add_u32 v247, %11, v247\n v_add_u32 v248, %11, v248\n"
        "v_add_u32 v249, %13, v249\n v_add_u32 v2, %12, v2\n v_add_u32 v3, 0x4400, v244\n v_add_u32 v230, %14, v230\n"
        "v_add_u32 v237, %15, v237\n v_add_u32 v238, %15, v238\n v_add3_u32 v239, %15, v239, %7"
        : "=&{v[208:239]}"(sp), "=&{v[128:135]}"(mi), "=&{v[244:251]}"(ta), "=&{v[0:7]}"(tb), "=&{v[240:243]}"(wf1)
        : "v"(sha), "v"(shb), "v"(h * 2304 - 1152), "s"(L_G1), "s"(((wq & 1) - 1) * 36864 + (wq >> 1) * 576), "s"((wq - 1) * 1536 + 8192),
          "s"((wq - 1) * 73728 + 36864), "s"((wq - 2) * 4096 + L_G1), "s"((wq >> 1) * 4608 + L_G1), "s"((wq + 1) * 8 * TS), "s"((wq - 3) * TS)
        : "memory");
    wait_vm<8>(); bar();
    issue_b(L, P, 0, wq);
    wait_vm<6>(); bar();
#pragma nounroll
    for (int s = 1; s < 3; ++s) issue_b(L, P, s, wq);
    raw = ssm_load(P, 32 * pg_m + (lane & 31));
    pre1();
#pragma nounroll
    for (int s = 3; s < 8; ++s) {
      wait_vm<12>();
      bar();
      issue_b(L, P, s, wq);
    }
    __builtin_amdgcn_s_setprio(3);
    wait_vm<12>(); bar();
    issue_b(L, P, 8, wq);
    const Cx lam = ssm_part1(raw);
    wait_vm<12>(); bar();
    const Cx l16p = ssm_part2(raw, lam, lpa);
    wait_vm<6>(); bar();
    asm volatile("global_load_dwordx2 %0, %3, %6 sc1\n global_load_dwordx2 %1, %4, %6 sc1\n global_load_dwordx2 %2, %5, %6 sc1"
                 : "=&v"(fv[0]), "=&v"(fv[1]), "=&v"(fv[2]) : "v"(fo[0]), "v"(fo[1]), "v"(fo[2]), "s"(ws) : "memory");
    ssm_part3(l16p, seg, lpa, lpb);
    wait_vm<3>(); bar();
    __builtin_amdgcn_s_setprio(0);
  }
  if (kh) {
    if (__builtin_expect(!isA, 0)) {
      fv[0] = 0; fv[1] = 1; fv[2] = 0;
    }
    asm volatile("s_waitcnt vmcnt(0)" : "+v"(fv[0]), "+v"(fv[1]), "+v"(fv[2]) :: "memory");
    if (__builtin_expect(__builtin_amdgcn_ballot_w64((fv[0] == tag) & (fv[1] == tag) & (fv[2] == tag)) != ~0ull, 0)) poll(ws, fo, tag);
    dma_tile(ws + head * 288 + (qb * 4 + wq) * (8 * QS), L_Q + wq * TS, qg);
#pragma unroll
    for (int i = 0; i < 2; ++i) dma_tile(kvg + (i * 32 + wq) * (4 * QS), 32 + (i * 32 + wq) * TS, wf1);
  }
  u32x32 cf1;
  u32x8 cf2;
  {
    const char *g = kvg + (kh * 32 + 4 + wq) * (4 * QS), *wfg = ws + wfo, *hp = ws + hq;
    unsigned ldst = 32 + (kh * 32 + 4 + wq) * TS, cnt;
    asm volatile(
    "s_waitcnt lgkmcnt(0)\n"
    "s_barrier\n"
    "ds_write_b32 v230, v231\n"
    "s_cmp_eq_u32 %[fh], 0\n"
    "s_cbranch_scc1 11f\n"
    "ds_read_b64 v[176:177], v236 offset:0\n"
    "ds_read_b64 v[178:179], v236 offset:256\n"
    "ds_read_b64 v[180:181], v236 offset:512\n"
    "ds_read_b64 v[182:183], v236 offset:768\n"
    "ds_read_b64 v[184:185], v236 offset:1024\n"
    "ds_read_b64 v[186:187], v236 offset:1280\n"
    "ds_read_b64 v[188:189], v235\n"
    "11:\n"
    "v_mov_b32 v8, 0\n"
    "v_mov_b32 v9, 0\n"
    "v_mov_b32 v10, 0\n"
    "v_mov_b32 v11, 0\n"
    "v_mov_b32 v12, 0\n"
    "v_mov_b32 v13, 0\n"
    "v_mov_b32 v14, 0\n"
    "v_mov_b32 v15, 0\n"
    "v_mov_b32 v16, 0\n"
    "v_mov_b32 v17, 0\n"
    "v_mov_b32 v18, 0\n"
    "v_mov_b32 v24, 0\n"
    "v_mov_b32 v25, 0\n"
    "v_mov_b32 v26, 0\n"
    "v_mov_b32 v27, 0\n"
    "v_mov_b32 v28, 0\n"
    "v_mov_b32 v29, 0\n"
    "v_mov_b32 v30, 0\n"
    "v_mov_b32 v31, 0\n"
    "v_mov_b32 v32, 0\n"
    "v_mov_b32 v33, 0\n"
    "v_mov_b32 v34, 0\n"
    "s_cbranch_scc1 11f\n"
    "v_mov_b32 v222, 0\n"
    "v_mov_b32 v223, 0\n"
    "s_waitcnt lgkmcnt(0)\n"
    "s_cmp_lt_u32 %[wq], 1\n"
    "s_cbranch_scc1 12f\n"
    "v_fmac_f32 v176, v214, v222\n"
    "v_fmac_f32 v177, v214, v223\n"
    "v_fma_f32 v176, -v215, v223, v176\n"
    "v_fmac_f32 v177, v215, v222\n"
    "v_fma_f32 v222, v214, v176, v178\n"
    "v_fma_f32 v223, v214, v177, v179\n"
    "v_fma_f32 v222, -v215, v177, v222\n"
    "v_fmac_f32 v223, v215, v176\n"
    "s_cmp_lt_u32 %[wq], 2\n"
    "s_cbranch_scc1 12f\n"
    "v_fmac_f32 v180, v214, v222\n"
    "v_fmac_f32 v181, v214, v223\n"
    "v_fma_f32 v180, -v215, v223, v180\n"
    "v_fmac_f32 v181, v215, v222\n"
    "v_fma_f32 v222, v214, v180, v182\n"
    "v_fma_f32 v223, v214, v181, v183\n"
    "v_fma_f32 v222, -v215, v181, v222\n"
    "v_fmac_f32 v223, v215, v180\n"
    "s_cmp_lt_u32 %[wq], 3\n"
    "s_cbranch_scc1 12f\n"
    "v_fmac_f32 v184, v214, v222\n"
    "v_fmac_f32 v185, v214, v223\n"
    "v_fma_f32 v184, -v215, v223, v184\n"
    "v_fmac_f32 v185, v215, v222\n"
    "v_fma_f32 v222, v214, v184, v186\n"
    "v_fma_f32 v223, v214, v185, v187\n"
    "v_fma_f32 v222, -v215, v185, v222\n"
    "v_fmac_f32 v223, v215, v184\n"
    "12:\n"
    "v_fmac_f32 v188, v214, v222\n"
    "v_fmac_f32 v189, v214, v223\n"
    "v_fma_f32 v188, -v215, v223, v188\n"
    "v_fmac_f32 v189, v215, v222\n"
    "s_mov_b32 exec_lo, 0\n"
    "v_mov_b32 v222, v188\n"
    "v_mov_b32 v223, v189\n"
    "s_mov_b32 exec_lo, -1\n"
    "s_cmp_lg_u32 %[wq], 3\n"
    "s_cbranch_scc1 11f\n"
    "v_mov_b32 v192, 0\n"
    "v_mov_b32 v194, 0\n"
    "v_mov_b32 v193, s46\n"
    "v_mov_b32 v195, s46\n"
    "s_mov_b32 exec_lo, 0\n"
    "v_fma_f32 v192, v214, v222, v226\n"
    "v_fma_f32 v194, v214, v223, v227\n"
    "v_fma_f32 v192, -v215, v223, v192\n"
    "v_fmac_f32 v194, v215, v222\n"
    "s_cmp_eq_u32 %[ne], 0\n"
    "s_cselect_b32 exec_lo, -1, 0\n"
    "s_waitcnt vmcnt(0)\n"
    "global_store_dwordx4 v233, v[192:195], %[ws] sc1\n"
    "s_mov_b32 exec_lo, -1\n"
    "s_branch 13f\n"
    "11:\n"
    "s_waitcnt vmcnt(0)\n"
    "13:\n"
    "s_waitcnt lgkmcnt(0)\n"
    "s_barrier\n"
    "ds_read_b64_tr_b16 v[120:121], v135 offset:0\n"
    "ds_read_b64_tr_b16 v[122:123], v135 offset:64\n"
    "ds_read_b64_tr_b16 v[104:105], v237 offset:0\n"
    "ds_read_b64_tr_b16 v[106:107], v237 offset:64\n"
    "ds_read_b64_tr_b16 v[108:109], v238 offset:0\n"
    "ds_read_b64_tr_b16 v[110:111], v238 offset:64\n"
    "ds_read_b64_tr_b16 v[114:115], v129 offset:0\n"
    "ds_read_b64_tr_b16 v[116:117], v129 offset:64\n"
    "ds_read_b64_tr_b16 v[112:113], v239 offset:0\n"
    "ds_read_b64_tr_b16 v[118:119], v130 offset:0\n"
    "s_waitcnt lgkmcnt(6)\n"
    "v_mfma_f32_32x32x16_bf16 v[72:87], v[120:123], v[104:107], 0\n"
    "s_waitcnt lgkmcnt(4)\n"
    "v_mfma_f32_32x32x16_bf16 v[88:103], v[120:123], v[108:111], 0\n"
    "s_waitcnt lgkmcnt(2)\n"
    "v_mfma_f32_32x32x16_bf16 v[40:55], v[114:117], v[104:107], 0\n"
    "ds_read_b128 v[120:123], v131 offset:0\n"
    "v_mfma_f32_32x32x16_bf16 v[56:71], v[114:117], v[108:111], 0\n"
    "ds_read_b64_tr_b16 v[114:115], v129 offset:2336\n"
    "ds_read_b64_tr_b16 v[116:117], v129 offset:2400\n"
    "s_mov_b32 m0, %[ldst]\n"
    "s_add_u32 %[ldst], %[ldst], 9344\n"
    "global_load_lds_dwordx4 v240, s[40:41] sc1\n"
    "global_load_lds_dwordx4 v241, s[40:41] offset:1024 sc1\n"
    "s_nop 3\n"
    "v_max3_f32 v124, v72, v73, v74\n"
    "v_max3_f32 v124, v124, v75, v76\n"
    "v_max3_f32 v124, v124, v77, v78\n"
    "v_max3_f32 v124, v124, v79, v80\n"
    "v_max3_f32 v124, v124, v81, v82\n"
    "v_max3_f32 v124, v124, v83, v84\n"
    "v_max3_f32 v124, v124, v85, v86\n"
    "v_max_f32 v124, v124, v87\n"
    "v_max3_f32 v125, v88, v89, v90\n"
    "v_max3_f32 v125, v125, v91, v92\n"
    "v_max3_f32 v125, v125, v93, v94\n"
    "v_max3_f32 v125, v125, v95, v96\n"
    "v_max3_f32 v125, v125, v97, v98\n"
    "v_max3_f32 v125, v125, v99, v100\n"
    "v_max3_f32 v125, v125, v101, v102\n"
    "v_max_f32 v125, v125, v103\n"
    "s_waitcnt lgkmcnt(0)\n"
    "s_nop 0\n"
    "v_permlane32_swap_b32 v124, v125\n"
    "s_nop 1\n"
    "v_max_f32 v124, v124, v125\n"
    "v_xor_b32 v124, 0x80000000, v124\n"
    "v_cvt_pk_bf16_f32 v113, v124, 0\n"
    "s_nop 1\n"
    "v_mfma_f32_32x32x4_2b_bf16 v[40:71], v[118:119], v[112:113], v[40:71]\n"
    "ds_read_b64_tr_b16 v[118:119], v130 offset:2336\n"
    "global_load_lds_dword v242, s[40:41] offset:2048 sc1\n"
    "s_add_u32 s40, s40, 442368\n"
    "s_addc_u32 s41, s41, 0\n"
    "s_nop 15\n"
    "s_nop 1\n"
    "s_waitcnt lgkmcnt(0)\n"
    "v_exp_f32 v40, v40\n"
    "v_exp_f32 v41, v41\n"
    "s_waitcnt lgkmcnt(2)\n"
    "ds_read_b128 v[124:127], v132 offset:0\n"
    "s_setprio 1\n"
    "v_exp_f32 v42, v42\n"
    "v_exp_f32 v43, v43\n"
    "v_exp_f32 v44, v44\n"
    "v_exp_f32 v45, v45\n"
    "v_mfma_f32_32x32x4_2b_bf16 v[72:103], v[118:119], v[112:113], 0\n"
    "v_exp_f32 v46, v46\n"
    "v_exp_f32 v47, v47\n"
    "v_exp_f32 v48, v48\n"
    "v_exp_f32 v49, v49\n"
    "v_exp_f32 v50, v50\n"
    "v_exp_f32 v51, v51\n"
    "v_exp_f32 v52, v52\n"
    "v_exp_f32 v53, v53\n"
    "v_smfmac_f32_32x32x32_bf16 v[8:23], v[120:123], v[40:47], v128\n"
    "s_waitcnt lgkmcnt(0)\n"
    "ds_read_b64_tr_b16 v[118:119], v130 offset:4672\n"
    "v_exp_f32 v54, v54\n"
    "v_exp_f32 v55, v55\n"
    "v_exp_f32 v56, v56\n"
    "v_exp_f32 v57, v57\n"
    "v_smfmac_f32_32x32x32_bf16 v[8:23], v[124:127], v[48:55], v128\n"
    "s_mov_b32 m0, %[ldst]\n"
    "s_add_u32 %[ldst], %[ldst], 9344\n"
    "global_load_lds_dwordx4 v240, s[40:41] sc1\n"
    "v_exp_f32 v58, v58\n"
    "v_exp_f32 v59, v59\n"
    "v_exp_f32 v60, v60\n"
    "v_exp_f32 v61, v61\n"
    "v_mfma_f32_32x32x16_bf16 v[72:87], v[114:117], v[104:107], v[72:87]\n"
    "global_load_lds_dwordx4 v241, s[40:41] offset:1024 sc1\n"
    "v_exp_f32 v62, v62\n"
    "v_exp_f32 v63, v63\n"
    "v_exp_f32 v64, v64\n"
    "v_exp_f32 v65, v65\n"
    "v_smfmac_f32_32x32x32_bf16 v[24:39], v[120:123], v[56:63], v128\n"
    "ds_read_b128 v[120:123], v131 offset:2336\n"
    "global_load_lds_dword v242, s[40:41] offset:2048 sc1\n"
    "s_add_u32 s40, s40, 442368\n"
    "s_addc_u32 s41, s41, 0\n"
    "v_exp_f32 v66, v66\n"
    "v_exp_f32 v67, v67\n"
    "v_exp_f32 v68, v68\n"
    "v_mfma_f32_32x32x16_bf16 v[88:103], v[114:117], v[108:111], v[88:103]\n"
    "ds_read_b64_tr_b16 v[114:115], v129 offset:4672\n"
    "ds_read_b64_tr_b16 v[116:117], v129 offset:4736\n"
    "v_exp_f32 v69, v69\n"
    "v_exp_f32 v70, v70\n"
    "v_exp_f32 v71, v71\n"
    "v_exp_f32 v72, v72\n"
    "v_exp_f32 v73, v73\n"
    "v_smfmac_f32_32x32x32_bf16 v[24:39], v[124:127], v[64:71], v128\n"
    "s_waitcnt lgkmcnt(2)\n"
    "ds_read_b128 v[124:127], v132 offset:2336\n"
    "s_setprio 0\n"
    "v_exp_f32 v74, v74\n"
    "v_exp_f32 v75, v75\n"
    "v_exp_f32 v76, v76\n"
    "v_exp_f32 v77, v77\n"
    "v_mfma_f32_32x32x4_2b_bf16 v[40:71], v[118:119], v[112:113], 0\n"
    "v_exp_f32 v78, v78\n"
    "v_exp_f32 v79, v79\n"
    "v_exp_f32 v80, v80\n"
    "v_exp_f32 v81, v81\n"
    "v_exp_f32 v82, v82\n"
    "v_exp_f32 v83, v83\n"
    "v_exp_f32 v84, v84\n"
    "v_exp_f32 v85, v85\n"
    "v_smfmac_f32_32x32x32_bf16 v[8:23], v[120:123], v[72:79], v128\n"
    "s_waitcnt lgkmcnt(0)\n"
    "ds_read_b64_tr_b16 v[118:119], v130 offset:7008\n"
    "v_exp_f32 v86, v86\n"
    "v_exp_f32 v87, v87\n"
    "v_exp_f32 v88, v88\n"
    "v_exp_f32 v89, v89\n"
    "v_smfmac_f32_32x32x32_bf16 v[8:23], v[124:127], v[80:87], v128\n"
    "s_mov_b32 m0, %[ldst]\n"
    "s_add_u32 %[ldst], %[ldst], 9344\n"
    "global_load_lds_dwordx4 v240, s[40:41] sc1\n"
    "v_exp_f32 v90, v90\n"
    "v_exp_f32 v91, v91\n"
    "v_exp_f32 v92, v92\n"
    "v_exp_f32 v93, v93\n"
    "v_mfma_f32_32x32x16_bf16 v[40:55], v[114:117], v[104:107], v[40:55]\n"
    "global_load_lds_dwordx4 v241, s[40:41] offset:1024 sc1\n"
    "v_exp_f32 v94, v94\n"
    "v_exp_f32 v95, v95\n"
    "v_exp_f32 v96, v96\n"
    "v_exp_f32 v97, v97\n"
    "v_smfmac_f32_32x32x32_bf16 v[24:39], v[120:123], v[88:95], v128\n"
    "ds_read_b128 v[120:123], v131 offset:4672\n"
    "global_load_lds_dword v242, s[40:41] offset:2048 sc1\n"
    "s_add_u32 s40, s40, 442368\n"
    "s_addc_u32 s41, s41, 0\n"
    "v_exp_f32 v98, v98\n"
    "v_exp_f32 v99, v99\n"
    "v_exp_f32 v100, v100\n"
    "v_mfma_f32_32x32x16_bf16 v[56:71], v[114:117], v[108:111], v[56:71]\n"
    "ds_read_b64_tr_b16 v[114:115], v129 offset:7008\n"
    "ds_read_b64_tr_b16 v[116:117], v129 offset:7072\n"
    "v_exp_f32 v101, v101\n"
    "v_exp_f32 v102, v102\n"
    "v_exp_f32 v103, v103\n"
    "v_exp_f32 v40, v40\n"
    "v_exp_f32 v41, v41\n"
    "v_smfmac_f32_32x32x32_bf16 v[24:39], v[124:127], v[96:103], v128\n"
    "s_waitcnt lgkmcnt(2)\n"
    "ds_read_b128 v[124:127], v132 offset:4672\n"
    "s_waitcnt vmcnt(6)\n"
    "s_barrier\n"
    "s_setprio 3\n"
    "v_exp_f32 v42, v42\n"
    "v_exp_f32 v43, v43\n"
    "v_exp_f32 v44, v44\n"
    "v_exp_f32 v45, v45\n"
    "v_mfma_f32_32x32x4_2b_bf16 v[72:103], v[118:119], v[112:113], 0\n"
    "v_exp_f32 v46, v46\n"
    "v_exp_f32 v47, v47\n"
    "v_exp_f32 v48, v48\n"
    "v_exp_f32 v49, v49\n"
    "v_exp_f32 v50, v50\n"
    "v_exp_f32 v51, v51\n"
    "v_exp_f32 v52, v52\n"
    "v_exp_f32 v53, v53\n"
    "v_smfmac_f32_32x32x32_bf16 v[8:23], v[120:123], v[40:47], v128\n"
    "s_waitcnt lgkmcnt(0)\n"
    "ds_read_b64_tr_b16 v[118:119], v130 offset:9344\n"
    "v_exp_f32 v54, v54\n"
    "v_exp_f32 v55, v55\n"
    "v_exp_f32 v56, v56\n"
    "v_exp_f32 v57, v57\n"
    "v_smfmac_f32_32x32x32_bf16 v[8:23], v[124:127], v[48:55], v128\n"
    "s_mov_b32 m0, %[ldst]\n"
    "s_add_u32 %[ldst], %[ldst], 9344\n"
    "global_load_lds_dwordx4 v240, s[40:41] sc1\n"
    "v_exp_f32 v58, v58\n"
    "v_exp_f32 v59, v59\n"
    "v_exp_f32 v60, v60\n"
    "v_exp_f32 v61, v61\n"
    "v_mfma_f32_32x32x16_bf16 v[72:87], v[114:117], v[104:107], v[72:87]\n"
    "global_load_lds_dwordx4 v241, s[40:41] offset:1024 sc1\n"
    "v_exp_f32 v62, v62\n"
    "v_exp_f32 v63, v63\n"
    "v_exp_f32 v64, v64\n"
    "v_exp_f32 v65, v65\n"
    "v_smfmac_f32_32x32x32_bf16 v[24:39], v[120:123], v[56:63], v128\n"
    "ds_read_b128 v[120:123], v131 offset:7008\n"
    "global_load_lds_dword v242, s[40:41] offset:2048 sc1\n"
    "s_add_u32 s40, s40, 442368\n"
    "s_addc_u32 s41, s41, 0\n"
    "v_exp_f32 v66, v66\n"
    "v_exp_f32 v67, v67\n"
    "v_exp_f32 v68, v68\n"
    "v_mfma_f32_32x32x16_bf16 v[88:103], v[114:117], v[108:111], v[88:103]\n"
    "ds_read_b64_tr_b16 v[114:115], v129 offset:9344\n"
    "ds_read_b64_tr_b16 v[116:117], v129 offset:9408\n"
    "v_exp_f32 v69, v69\n"
    "v_exp_f32 v70, v70\n"
    "v_exp_f32 v71, v71\n"
    "v_exp_f32 v72, v72\n"
    "v_exp_f32 v73, v73\n"
    "v_smfmac_f32_32x32x32_bf16 v[24:39], v[124:127], v[64:71], v128\n"
    "s_waitcnt lgkmcnt(2)\n"
    "ds_read_b128 v[124:127], v132 offset:7008\n"
    "s_setprio 2\n"
    "v_exp_f32 v74, v74\n"
    "v_exp_f32 v75, v75\n"
    "v_exp_f32 v76, v76\n"
    "v_exp_f32 v77, v77\n"
    "v_mfma_f32_32x32x4_2b_bf16 v[40:71], v[118:119], v[112:113], 0\n"
    "v_exp_f32 v78, v78\n"
    "v_exp_f32 v79, v79\n"
    "v_exp_f32 v80, v80\n"
    "v_exp_f32 v81, v81\n"
    "v_exp_f32 v82, v82\n"
    "v_exp_f32 v83, v83\n"
    "v_exp_f32 v84, v84\n"
    "v_exp_f32 v85, v85\n"
    "v_smfmac_f32_32x32x32_bf16 v[8:23], v[120:123], v[72:79], v128\n"
    "s_waitcnt lgkmcnt(0)\n"
    "ds_read_b64_tr_b16 v[118:119], v130 offset:11680\n"
    "v_exp_f32 v86, v86\n"
    "v_exp_f32 v87, v87\n"
    "v_exp_f32 v88, v88\n"
    "v_exp_f32 v89, v89\n"
    "v_smfmac_f32_32x32x32_bf16 v[8:23], v[124:127], v[80:87], v128\n"
    "s_mov_b32 m0, %[ldst]\n"
    "s_add_u32 %[ldst], %[ldst], 9344\n"
    "global_load_lds_dwordx4 v240, s[40:41] sc1\n"
    "v_exp_f32 v90, v90\n"
    "v_exp_f32 v91, v91\n"
    "v_exp_f32 v92, v92\n"
    "v_exp_f32 v93, v93\n"
    "v_mfma_f32_32x32x16_bf16 v[40:55], v[114:117], v[104:107], v[40:55]\n"
    "global_load_lds_dwordx4 v241, s[40:41] offset:1024 sc1\n"
    "v_exp_f32 v94, v94\n"
    "v_exp_f32 v95, v95\n"
    "v_exp_f32 v96, v96\n"
    "v_exp_f32 v97, v97\n"
    "v_smfmac_f32_32x32x32_bf16 v[24:39], v[120:123], v[88:95], v128\n"
    "ds_read_b128 v[120:123], v131 offset:9344\n"
    "global_load_lds_dword v242, s[40:41] offset:2048 sc1\n"
    "s_add_u32 s40, s40, 442368\n"
    "s_addc_u32 s41, s41, 0\n"
    "v_exp_f32 v98, v98\n"
    "v_exp_f32 v99, v99\n"
    "v_exp_f32 v100, v100\n"
    "v_mfma_f32_32x32x16_bf16 v[56:71], v[114:117], v[108:111], v[56:71]\n"
    "ds_read_b64_tr_b16 v[114:115], v129 offset:11680\n"
    "ds_read_b64_tr_b16 v[116:117], v129 offset:11744\n"
    "v_exp_f32 v101, v101\n"
    "v_exp_f32 v102, v102\n"
    "v_exp_f32 v103, v103\n"
    "v_exp_f32 v40, v40\n"
    "v_exp_f32 v41, v41\n"
    "v_smfmac_f32_32x32x32_bf16 v[24:39], v[124:127], v[96:103], v128\n"
    "s_waitcnt lgkmcnt(2)\n"
    "ds_read_b128 v[124:127], v132 offset:9344\n"
    "s_setprio 1\n"
    "v_exp_f32 v42, v42\n"
    "v_exp_f32 v43, v43\n"
    "v_exp_f32 v44, v44\n"
    "v_exp_f32 v45, v45\n"
    "v_mfma_f32_32x32x4_2b_bf16 v[72:103], v[118:119], v[112:113], 0\n"
    "v_exp_f32 v46, v46\n"
    "v_exp_f32 v47, v47\n"
    "v_exp_f32 v48, v48\n"
    "v_exp_f32 v49, v49\n"
    "v_exp_f32 v50, v50\n"
    "v_exp_f32 v51, v51\n"
    "v_exp_f32 v52, v52\n"
    "v_exp_f32 v53, v53\n"
    "v_smfmac_f32_32x32x32_bf16 v[8:23], v[120:123], v[40:47], v128\n"
    "s_waitcnt lgkmcnt(0)\n"
    "ds_read_b64_tr_b16 v[118:119], v130 offset:14016\n"
    "v_exp_f32 v54, v54\n"
    "v_exp_f32 v55, v55\n"
    "v_exp_f32 v56, v56\n"
    "v_exp_f32 v57, v57\n"
    "v_smfmac_f32_32x32x32_bf16 v[8:23], v[124:127], v[48:55], v128\n"
    "s_mov_b32 m0, %[ldst]\n"
    "s_add_u32 %[ldst], %[ldst], 9344\n"
    "global_load_lds_dwordx4 v240, s[40:41] sc1\n"
    "v_exp_f32 v58, v58\n"
    "v_exp_f32 v59, v59\n"
    "v_exp_f32 v60, v60\n"
    "v_exp_f32 v61, v61\n"
    "v_mfma_f32_32x32x16_bf16 v[72:87], v[114:117], v[104:107], v[72:87]\n"
    "global_load_lds_dwordx4 v241, s[40:41] offset:1024 sc1\n"
    "v_exp_f32 v62, v62\n"
    "v_exp_f32 v63, v63\n"
    "v_exp_f32 v64, v64\n"
    "v_exp_f32 v65, v65\n"
    "v_smfmac_f32_32x32x32_bf16 v[24:39], v[120:123], v[56:63], v128\n"
    "ds_read_b128 v[120:123], v131 offset:11680\n"
    "global_load_lds_dword v242, s[40:41] offset:2048 sc1\n"
    "s_add_u32 s40, s40, 442368\n"
    "s_addc_u32 s41, s41, 0\n"
    "v_exp_f32 v66, v66\n"
    "v_exp_f32 v67, v67\n"
    "v_exp_f32 v68, v68\n"
    "v_mfma_f32_32x32x16_bf16 v[88:103], v[114:117], v[108:111], v[88:103]\n"
    "ds_read_b64_tr_b16 v[114:115], v129 offset:14016\n"
    "ds_read_b64_tr_b16 v[116:117], v129 offset:14080\n"
    "v_exp_f32 v69, v69\n"
    "v_exp_f32 v70, v70\n"
    "v_exp_f32 v71, v71\n"
    "v_exp_f32 v72, v72\n"
    "v_exp_f32 v73, v73\n"
    "v_smfmac_f32_32x32x32_bf16 v[24:39], v[124:127], v[64:71], v128\n"
    "s_waitcnt lgkmcnt(2)\n"
    "ds_read_b128 v[124:127], v132 offset:11680\n"
    "s_setprio 0\n"
    "v_exp_f32 v74, v74\n"
    "v_exp_f32 v75, v75\n"
    "v_exp_f32 v76, v76\n"
    "v_exp_f32 v77, v77\n"
    "v_mfma_f32_32x32x4_2b_bf16 v[40:71], v[118:119], v[112:113], 0\n"
    "v_exp_f32 v78, v78\n"
    "v_exp_f32 v79, v79\n"
    "v_exp_f32 v80, v80\n"
    "v_exp_f32 v81, v81\n"
    "v_exp_f32 v82, v82\n"
    "v_exp_f32 v83, v83\n"
    "v_exp_f32 v84, v84\n"
    "v_exp_f32 v85, v85\n"
    "v_smfmac_f32_32x32x32_bf16 v[8:23], v[120:123], v[72:79], v128\n"
    "s_waitcnt lgkmcnt(0)\n"
    "ds_read_b64_tr_b16 v[118:119], v130 offset:16352\n"
    "v_exp_f32 v86, v86\n"
    "v_exp_f32 v87, v87\n"
    "v_exp_f32 v88, v88\n"
    "v_exp_f32 v89, v89\n"
    "v_smfmac_f32_32x32x32_bf16 v[8:23], v[124:127], v[80:87], v128\n"
    "s_mov_b32 m0, %[ldst]\n"
    "s_add_u32 %[ldst], %[ldst], 9344\n"
    "global_load_lds_dwordx4 v240, s[40:41] sc1\n"
    "v_exp_f32 v90, v90\n"
    "v_exp_f32 v91, v91\n"
    "v_exp_f32 v92, v92\n"
    "v_exp_f32 v93, v93\n"
    "v_mfma_f32_32x32x16_bf16 v[40:55], v[114:117], v[104:107], v[40:55]\n"
    "global_load_lds_dwordx4 v241, s[40:41] offset:1024 sc1\n"
    "v_exp_f32 v94, v94\n"
    "v_exp_f32 v95, v95\n"
    "v_exp_f32 v96, v96\n"
    "v_exp_f32 v97, v97\n"
    "v_smfmac_f32_32x32x32_bf16 v[24:39], v[120:123], v[88:95], v128\n"
    "ds_read_b128 v[120:123], v131 offset:14016\n"
    "global_load_lds_dword v242, s[40:41] offset:2048 sc1\n"
    "s_add_u32 s40, s40, 442368\n"
    "s_addc_u32 s41, s41, 0\n"
    "v_exp_f32 v98, v98\n"
    "v_exp_f32 v99, v99\n"
    "v_exp_f32 v100, v100\n"
    "v_mfma_f32_32x32x16_bf16 v[56:71], v[114:117], v[108:111], v[56:71]\n"
    "ds_read_b64_tr_b16 v[114:115], v129 offset:16352\n"
    "ds_read_b64_tr_b16 v[116:117], v129 offset:16416\n"
    "v_exp_f32 v101, v101\n"
    "v_exp_f32 v102, v102\n"
    "v_exp_f32 v103, v103\n"
    "v_exp_f32 v40, v40\n"
    "v_exp_f32 v41, v41\n"
    "v_smfmac_f32_32x32x32_bf16 v[24:39], v[124:127], v[96:103], v128\n"
    "s_waitcnt lgkmcnt(2)\n"
    "ds_read_b128 v[124:127], v132 offset:14016\n"
    "s_waitcnt vmcnt(6)\n"
    "s_barrier\n"
    "s_setprio 1\n"
    "v_exp_f32 v42, v42\n"
    "v_exp_f32 v43, v43\n"
    "v_exp_f32 v44, v44\n"
    "v_exp_f32 v45, v45\n"
    "v_mfma_f32_32x32x4_2b_bf16 v[72:103], v[118:119], v[112:113], 0\n"
    "v_exp_f32 v46, v46\n"
    "v_exp_f32 v47, v47\n"
    "v_exp_f32 v48, v48\n"
    "v_exp_f32 v49, v49\n"
    "v_exp_f32 v50, v50\n"
    "v_exp_f32 v51, v51\n"
    "v_exp_f32 v52, v52\n"
    "v_exp_f32 v53, v53\n"
    "v_smfmac_f32_32x32x32_bf16 v[8:23], v[120:123], v[40:47], v128\n"
    "s_waitcnt lgkmcnt(0)\n"
    "ds_read_b64_tr_b16 v[118:119], v130 offset:18688\n"
    "v_exp_f32 v54, v54\n"
    "v_exp_f32 v55, v55\n"
    "v_exp_f32 v56, v56\n"
    "v_exp_f32 v57, v57\n"
    "v_smfmac_f32_32x32x32_bf16 v[8:23], v[124:127], v[48:55], v128\n"
    "s_cmp_eq_u32 %[ne], 0\n"
    "s_cbranch_scc1 4f\n"
    "global_load_dwordx4 v[176:179], v232, %[ws] offset:3072 sc1\n"
    "global_load_dwordx4 v[180:183], v232, %[ws] offset:2048 sc1\n"
    "global_load_dwordx4 v[184:187], v232, %[ws] offset:1024 sc1\n"
    "global_load_dwordx4 v[188:191], v232, %[ws] offset:0 sc1\n"
    "4:\n"
    "v_exp_f32 v58, v58\n"
    "v_exp_f32 v59, v59\n"
    "v_exp_f32 v60, v60\n"
    "v_exp_f32 v61, v61\n"
    "v_mfma_f32_32x32x16_bf16 v[72:87], v[114:117], v[104:107], v[72:87]\n"
    "v_exp_f32 v62, v62\n"
    "v_exp_f32 v63, v63\n"
    "v_exp_f32 v64, v64\n"
    "v_exp_f32 v65, v65\n"
    "v_smfmac_f32_32x32x32_bf16 v[24:39], v[120:123], v[56:63], v128\n"
    "ds_read_b128 v[120:123], v131 offset:16352\n"
    "s_cmp_lt_u32 %[ne], 5\n"
    "s_cbranch_scc1 4f\n"
    "global_load_dwordx4 v[192:195], v232, %[ws] offset:-1024 sc1\n"
    "global_load_dwordx4 v[196:199], v232, %[ws] offset:-2048 sc1\n"
    "global_load_dwordx4 v[200:203], v232, %[ws] offset:-3072 sc1\n"
    "global_load_dwordx4 v[204:207], v232, %[ws] offset:-4096 sc1\n"
    "4:\n"
    "v_exp_f32 v66, v66\n"
    "v_exp_f32 v67, v67\n"
    "v_exp_f32 v68, v68\n"
    "v_mfma_f32_32x32x16_bf16 v[88:103], v[114:117], v[108:111], v[88:103]\n"
    "ds_read_b64_tr_b16 v[114:115], v129 offset:18688\n"
    "ds_read_b64_tr_b16 v[116:117], v129 offset:18752\n"
    "v_exp_f32 v69, v69\n"
    "v_exp_f32 v70, v70\n"
    "v_exp_f32 v71, v71\n"
    "v_exp_f32 v72, v72\n"
    "v_exp_f32 v73, v73\n"
    "v_smfmac_f32_32x32x32_bf16 v[24:39], v[124:127], v[64:71], v128\n"
    "s_waitcnt lgkmcnt(2)\n"
    "ds_read_b128 v[124:127], v132 offset:16352\n"
    "s_setprio 0\n"
    "v_exp_f32 v74, v74\n"
    "v_exp_f32 v75, v75\n"
    "v_exp_f32 v76, v76\n"
    "v_exp_f32 v77, v77\n"
    "v_mfma_f32_32x32x4_2b_bf16 v[40:71], v[118:119], v[112:113], 0\n"
    "v_exp_f32 v78, v78\n"
    "v_exp_f32 v79, v79\n"
    "v_exp_f32 v80, v80\n"
    "v_exp_f32 v81, v81\n"
    "v_exp_f32 v82, v82\n"
    "v_exp_f32 v83, v83\n"
    "v_exp_f32 v84, v84\n"
    "v_exp_f32 v85, v85\n"
    "v_smfmac_f32_32x32x32_bf16 v[8:23], v[120:123], v[72:79], v128\n"
    "s_waitcnt lgkmcnt(0)\n"
    "ds_read_b64_tr_b16 v[118:119], v130 offset:21024\n"
    "v_exp_f32 v86, v86\n"
    "v_exp_f32 v87, v87\n"
    "v_exp_f32 v88, v88\n"
    "v_exp_f32 v89, v89\n"
    "v_smfmac_f32_32x32x32_bf16 v[8:23], v[124:127], v[80:87], v128\n"
    "v_exp_f32 v90, v90\n"
    "v_exp_f32 v91, v91\n"
    "v_exp_f32 v92, v92\n"
    "v_exp_f32 v93, v93\n"
    "v_mfma_f32_32x32x16_bf16 v[40:55], v[114:117], v[104:107], v[40:55]\n"
    "v_exp_f32 v94, v94\n"
    "v_exp_f32 v95, v95\n"
    "v_exp_f32 v96, v96\n"
    "v_exp_f32 v97, v97\n"
    "v_smfmac_f32_32x32x32_bf16 v[24:39], v[120:123], v[88:95], v128\n"
    "ds_read_b128 v[120:123], v131 offset:18688\n"
    "v_exp_f32 v98, v98\n"
    "v_exp_f32 v99, v99\n"
    "v_exp_f32 v100, v100\n"
    "v_mfma_f32_32x32x16_bf16 v[56:71], v[114:117], v[108:111], v[56:71]\n"
    "ds_read_b64_tr_b16 v[114:115], v129 offset:21024\n"
    "ds_read_b64_tr_b16 v[116:117], v129 offset:21088\n"
    "v_exp_f32 v101, v101\n"
    "v_exp_f32 v102, v102\n"
    "v_exp_f32 v103, v103\n"
    "v_add_u32 v129, 18688, v129\n"
    "v_add_u32 v130, 18688, v130\n"
    "v_add_u32 v131, 18688, v131\n"
    "v_add_u32 v132, 18688, v132\n"
    "s_waitcnt vmcnt(0)\n"
    "s_barrier\n"
    "s_cmp_eq_u32 %[fh], 0\n"
    "s_cbranch_scc1 20f\n"
    "s_setprio 3\n"
    "s_cmp_eq_u32 %[ne], 0\n"
    "s_cbranch_scc1 23f\n"
    "s_mov_b32 %[cnt], 0x40000\n"
    "21:\n"
    "v_mov_b32 v224, s46\n"
    "v_mov_b32 v225, s46\n"
    "s_getpc_b64 s[48:49]\n"
    "s_add_u32 s48, s48, %[jo]\n"
    "s_addc_u32 s49, s49, 0\n"
    "s_setpc_b64 s[48:49]\n"
    "v_min3_u32 v224, v224, v205, v207\n"
    "v_max3_u32 v225, v225, v205, v207\n"
    "v_fmac_f32 v200, v218, v204\n"
    "v_fmac_f32 v202, v218, v206\n"
    "v_fma_f32 v200, -v219, v206, v200\n"
    "v_fmac_f32 v202, v219, v204\n"
    "v_min3_u32 v224, v224, v201, v203\n"
    "v_max3_u32 v225, v225, v201, v203\n"
    "v_fmac_f32 v196, v218, v200\n"
    "v_fmac_f32 v198, v218, v202\n"
    "v_fma_f32 v196, -v219, v202, v196\n"
    "v_fmac_f32 v198, v219, v200\n"
    "v_min3_u32 v224, v224, v197, v199\n"
    "v_max3_u32 v225, v225, v197, v199\n"
    "v_fmac_f32 v192, v218, v196\n"
    "v_fmac_f32 v194, v218, v198\n"
    "v_fma_f32 v192, -v219, v198, v192\n"
    "v_fmac_f32 v194, v219, v196\n"
    "v_min3_u32 v224, v224, v193, v195\n"
    "v_max3_u32 v225, v225, v193, v195\n"
    "v_fmac_f32 v188, v218, v192\n"
    "v_fmac_f32 v190, v218, v194\n"
    "v_fma_f32 v188, -v219, v194, v188\n"
    "v_fmac_f32 v190, v219, v192\n"
    "v_min3_u32 v224, v224, v189, v191\n"
    "v_max3_u32 v225, v225, v189, v191\n"
    "v_fmac_f32 v184, v218, v188\n"
    "v_fmac_f32 v186, v218, v190\n"
    "v_fma_f32 v184, -v219, v190, v184\n"
    "v_fmac_f32 v186, v219, v188\n"
    "v_min3_u32 v224, v224, v185, v187\n"
    "v_max3_u32 v225, v225, v185, v187\n"
    "v_fmac_f32 v180, v218, v184\n"
    "v_fmac_f32 v182, v218, v186\n"
    "v_fma_f32 v180, -v219, v186, v180\n"
    "v_fmac_f32 v182, v219, v184\n"
    "v_min3_u32 v224, v224, v181, v183\n"
    "v_max3_u32 v225, v225, v181, v183\n"
    "v_fmac_f32 v176, v218, v180\n"
    "v_fmac_f32 v178, v218, v182\n"
    "v_fma_f32 v176, -v219, v182, v176\n"
    "v_fmac_f32 v178, v219, v180\n"
    "v_min3_u32 v224, v224, v177, v179\n"
    "v_max3_u32 v225, v225, v177, v179\n"
    "v_cmp_ne_u32 vcc, v224, v225\n"
    "s_cbranch_vccz 22f\n"
    "s_sub_u32 %[cnt], %[cnt], 1\n"
    "s_cbranch_scc1 22f\n"
    "s_sleep 2\n"
    "global_load_dwordx4 v[176:179], v232, %[ws] offset:3072 sc1\n"
    "global_load_dwordx4 v[180:183], v232, %[ws] offset:2048 sc1\n"
    "global_load_dwordx4 v[184:187], v232, %[ws] offset:1024 sc1\n"
    "global_load_dwordx4 v[188:191], v232, %[ws] offset:0 sc1\n"
    "global_load_dwordx4 v[192:195], v232, %[ws] offset:-1024 sc1\n"
    "global_load_dwordx4 v[196:199], v232, %[ws] offset:-2048 sc1\n"
    "global_load_dwordx4 v[200:203], v232, %[ws] offset:-3072 sc1\n"
    "global_load_dwordx4 v[204:207], v232, %[ws] offset:-4096 sc1\n"
    "s_waitcnt vmcnt(0)\n"
    "s_branch 21b\n"
    "22:\n"
    "v_mov_b32 v224, v176\n"
    "v_mov_b32 v225, v178\n"
    "s_nop 0\n"
    "v_permlane32_swap_b32 v176, v224\n"
    "v_permlane32_swap_b32 v178, v225\n"
    "s_nop 1\n"
    "v_fmac_f32 v176, v216, v224\n"
    "v_fmac_f32 v178, v216, v225\n"
    "v_fma_f32 v176, -v217, v225, v176\n"
    "v_fmac_f32 v178, v217, v224\n"
    "v_fmac_f32 v222, v220, v176\n"
    "v_fmac_f32 v223, v220, v178\n"
    "v_fma_f32 v222, -v221, v178, v222\n"
    "v_fmac_f32 v223, v221, v176\n"
    "23:\n"
    "s_mov_b64 s[48:49], %[ws]\n"
    "v_fmac_f32 v136, v208, v222\n"
    "v_fmac_f32 v152, v208, v223\n"
    "v_fmac_f32 v136, v210, v223\n"
    "v_fmac_f32 v152, v209, v222\n"
    "v_fmac_f32 v137, v208, v136\n"
    "v_fmac_f32 v153, v208, v152\n"
    "v_fmac_f32 v137, v210, v152\n"
    "v_fmac_f32 v153, v209, v136\n"
    "v_cvt_pk_bf16_f32 v176, v136, v137\n"
    "v_cvt_pk_bf16_f32 v180, v152, v153\n"
    "v_fmac_f32 v138, v208, v137\n"
    "v_fmac_f32 v154, v208, v153\n"
    "v_fmac_f32 v138, v210, v153\n"
    "v_fmac_f32 v154, v209, v137\n"
    "v_fmac_f32 v139, v208, v138\n"
    "v_fmac_f32 v155, v208, v154\n"
    "v_fmac_f32 v139, v210, v154\n"
    "v_fmac_f32 v155, v209, v138\n"
    "v_cvt_pk_bf16_f32 v177, v138, v139\n"
    "v_cvt_pk_bf16_f32 v181, v154, v155\n"
    "v_fmac_f32 v140, v208, v139\n"
    "v_fmac_f32 v156, v208, v155\n"
    "v_fmac_f32 v140, v210, v155\n"
    "v_fmac_f32 v156, v209, v139\n"
    "v_fmac_f32 v141, v208, v140\n"
    "v_fmac_f32 v157, v208, v156\n"
    "v_fmac_f32 v141, v210, v156\n"
    "v_fmac_f32 v157, v209, v140\n"
    "v_cvt_pk_bf16_f32 v178, v140, v141\n"
    "v_cvt_pk_bf16_f32 v182, v156, v157\n"
    "v_fmac_f32 v142, v208, v141\n"
    "v_fmac_f32 v158, v208, v157\n"
    "v_fmac_f32 v142, v210, v157\n"
    "v_fmac_f32 v158, v209, v141\n"
    "v_fmac_f32 v143, v208, v142\n"
    "v_fmac_f32 v159, v208, v158\n"
    "v_fmac_f32 v143, v210, v158\n"
    "v_fmac_f32 v159, v209, v142\n"
    "v_cvt_pk_bf16_f32 v179, v142, v143\n"
    "v_cvt_pk_bf16_f32 v183, v158, v159\n"
    "global_store_dwordx4 v234, v[176:179], s[48:49] sc1\n"
    "s_add_u32 s48, s48, 9216\n"
    "s_addc_u32 s49, s49, 0\n"
    "global_store_dwordx4 v234, v[180:183], s[48:49] sc1\n"
    "s_add_u32 s48, s48, 9216\n"
    "s_addc_u32 s49, s49, 0\n"
    "v_fmac_f32 v144, v208, v143\n"
    "v_fmac_f32 v160, v208, v159\n"
    "v_fmac_f32 v144, v210, v159\n"
    "v_fmac_f32 v160, v209, v143\n"
    "v_fmac_f32 v145, v208, v144\n"
    "v_fmac_f32 v161, v208, v160\n"
    "v_fmac_f32 v145, v210, v160\n"
    "v_fmac_f32 v161, v209, v144\n"
    "v_cvt_pk_bf16_f32 v184, v144, v145\n"
    "v_cvt_pk_bf16_f32 v188, v160, v161\n"
    "v_fmac_f32 v146, v208, v145\n"
    "v_fmac_f32 v162, v208, v161\n"
    "v_fmac_f32 v146, v210, v161\n"
    "v_fmac_f32 v162, v209, v145\n"
    "v_fmac_f32 v147, v208, v146\n"
    "v_fmac_f32 v163, v208, v162\n"
    "v_fmac_f32 v147, v210, v162\n"
    "v_fmac_f32 v163, v209, v146\n"
    "v_cvt_pk_bf16_f32 v185, v146, v147\n"
    "v_cvt_pk_bf16_f32 v189, v162, v163\n"
    "v_fmac_f32 v148, v208, v147\n"
    "v_fmac_f32 v164, v208, v163\n"
    "v_fmac_f32 v148, v210, v163\n"
    "v_fmac_f32 v164, v209, v147\n"
    "v_fmac_f32 v149, v208, v148\n"
    "v_fmac_f32 v165, v208, v164\n"
    "v_fmac_f32 v149, v210, v164\n"
    "v_fmac_f32 v165, v209, v148\n"
    "v_cvt_pk_bf16_f32 v186, v148, v149\n"
    "v_cvt_pk_bf16_f32 v190, v164, v165\n"
    "v_fmac_f32 v150, v208, v149\n"
    "v_fmac_f32 v166, v208, v165\n"
    "v_fmac_f32 v150, v210, v165\n"
    "v_fmac_f32 v166, v209, v149\n"
    "v_fmac_f32 v151, v208, v150\n"
    "v_fmac_f32 v167, v208, v166\n"
    "v_fmac_f32 v151, v210, v166\n"
    "v_fmac_f32 v167, v209, v150\n"
    "v_cvt_pk_bf16_f32 v187, v150, v151\n"
    "v_cvt_pk_bf16_f32 v191, v166, v167\n"
    "global_store_dwordx4 v234, v[184:187], s[48:49] sc1\n"
    "s_add_u32 s48, s48, 9216\n"
    "s_addc_u32 s49, s49, 0\n"
    "global_store_dwordx4 v234, v[188:191], s[48:49] sc1\n"
    "20:\n"
    "s_mov_b64 s[48:49], %[cg]\n"
    "v_exp_f32 v40, v40\n"
    "v_exp_f32 v41, v41\n"
    "v_smfmac_f32_32x32x32_bf16 v[24:39], v[124:127], v[96:103], v128\n"
    "s_waitcnt lgkmcnt(2)\n"
    "ds_read_b128 v[124:127], v132 offset:0\n"
    "s_setprio 2\n"
    "v_exp_f32 v42, v42\n"
    "v_exp_f32 v43, v43\n"
    "v_exp_f32 v44, v44\n"
    "v_exp_f32 v45, v45\n"
    "v_mfma_f32_32x32x4_2b_bf16 v[72:103], v[118:119], v[112:113], 0\n"
    "v_exp_f32 v46, v46\n"
    "v_exp_f32 v47, v47\n"
    "v_exp_f32 v48, v48\n"
    "v_exp_f32 v49, v49\n"
    "v_exp_f32 v50, v50\n"
    "v_exp_f32 v51, v51\n"
    "v_exp_f32 v52, v52\n"
    "v_exp_f32 v53, v53\n"
    "v_smfmac_f32_32x32x32_bf16 v[8:23], v[120:123], v[40:47], v128\n"
    "s_waitcnt lgkmcnt(0)\n"
    "ds_read_b64_tr_b16 v[118:119], v130 offset:4672\n"
    "v_exp_f32 v54, v54\n"
    "v_exp_f32 v55, v55\n"
    "v_exp_f32 v56, v56\n"
    "v_exp_f32 v57, v57\n"
    "v_smfmac_f32_32x32x32_bf16 v[8:23], v[124:127], v[48:55], v128\n"
    "s_add_u32 m0, %[lc], 0\n"
    "s_nop 0\n"
    "global_load_lds_dwordx4 v244, s[48:49]\n"
    "v_exp_f32 v58, v58\n"
    "v_exp_f32 v59, v59\n"
    "v_exp_f32 v60, v60\n"
    "v_exp_f32 v61, v61\n"
    "v_mfma_f32_32x32x16_bf16 v[72:87], v[114:117], v[104:107], v[72:87]\n"
    "v_exp_f32 v62, v62\n"
    "v_exp_f32 v63, v63\n"
    "v_exp_f32 v64, v64\n"
    "v_exp_f32 v65, v65\n"
    "v_smfmac_f32_32x32x32_bf16 v[24:39], v[120:123], v[56:63], v128\n"
    "ds_read_b128 v[120:123], v131 offset:2336\n"
    "global_load_lds_dwordx4 v3, s[48:49] offset:1024\n"
    "s_add_u32 s48, s48, 64\n"
    "s_addc_u32 s49, s49, 0\n"
    "v_exp_f32 v66, v66\n"
    "v_exp_f32 v67, v67\n"
    "v_exp_f32 v68, v68\n"
    "v_mfma_f32_32x32x16_bf16 v[88:103], v[114:117], v[108:111], v[88:103]\n"
    "ds_read_b64_tr_b16 v[114:115], v129 offset:4672\n"
    "ds_read_b64_tr_b16 v[116:117], v129 offset:4736\n"
    "v_exp_f32 v69, v69\n"
    "v_exp_f32 v70, v70\n"
    "v_exp_f32 v71, v71\n"
    "v_exp_f32 v72, v72\n"
    "v_exp_f32 v73, v73\n"
    "v_smfmac_f32_32x32x32_bf16 v[24:39], v[124:127], v[64:71], v128\n"
    "s_waitcnt lgkmcnt(2)\n"
    "ds_read_b128 v[124:127], v132 offset:2336\n"
    "v_exp_f32 v74, v74\n"
    "v_exp_f32 v75, v75\n"
    "v_exp_f32 v76, v76\n"
    "v_exp_f32 v77, v77\n"
    "v_mfma_f32_32x32x4_2b_bf16 v[40:71], v[118:119], v[112:113], 0\n"
    "v_exp_f32 v78, v78\n"
    "v_exp_f32 v79, v79\n"
    "v_exp_f32 v80, v80\n"
    "v_exp_f32 v81, v81\n"
    "v_exp_f32 v82, v82\n"
    "v_exp_f32 v83, v83\n"
    "v_exp_f32 v84, v84\n"
    "v_exp_f32 v85, v85\n"
    "v_smfmac_f32_32x32x32_bf16 v[8:23], v[120:123], v[72:79], v128\n"
    "s_waitcnt lgkmcnt(0)\n"
    "ds_read_b64_tr_b16 v[118:119], v130 offset:7008\n"
    "v_exp_f32 v86, v86\n"
    "v_exp_f32 v87, v87\n"
    "v_exp_f32 v88, v88\n"
    "v_exp_f32 v89, v89\n"
    "v_smfmac_f32_32x32x32_bf16 v[8:23], v[124:127], v[80:87], v128\n"
    "s_add_u32 m0, %[lc], 2048\n"
    "s_nop 0\n"
    "global_load_lds_dwordx4 v244, s[48:49]\n"
    "v_exp_f32 v90, v90\n"
    "v_exp_f32 v91, v91\n"
    "v_exp_f32 v92, v92\n"
    "v_exp_f32 v93, v93\n"
    "v_mfma_f32_32x32x16_bf16 v[40:55], v[114:117], v[104:107], v[40:55]\n"
    "v_exp_f32 v94, v94\n"
    "v_exp_f32 v95, v95\n"
    "v_exp_f32 v96, v96\n"
    "v_exp_f32 v97, v97\n"
    "v_smfmac_f32_32x32x32_bf16 v[24:39], v[120:123], v[88:95], v128\n"
    "ds_read_b128 v[120:123], v131 offset:4672\n"
    "global_load_lds_dwordx4 v3, s[48:49] offset:1024\n"
    "s_add_u32 s48, s48, 64\n"
    "s_addc_u32 s49, s49, 0\n"
    "v_exp_f32 v98, v98\n"
    "v_exp_f32 v99, v99\n"
    "v_exp_f32 v100, v100\n"
    "v_mfma_f32_32x32x16_bf16 v[56:71], v[114:117], v[108:111], v[56:71]\n"
    "ds_read_b64_tr_b16 v[114:115], v129 offset:7008\n"
    "ds_read_b64_tr_b16 v[116:117], v129 offset:7072\n"
    "v_exp_f32 v101, v101\n"
    "v_exp_f32 v102, v102\n"
    "v_exp_f32 v103, v103\n"
    "v_exp_f32 v40, v40\n"
    "v_exp_f32 v41, v41\n"
    "v_smfmac_f32_32x32x32_bf16 v[24:39], v[124:127], v[96:103], v128\n"
    "s_waitcnt lgkmcnt(2)\n"
    "ds_read_b128 v[124:127], v132 offset:4672\n"
    "v_exp_f32 v42, v42\n"
    "v_exp_f32 v43, v43\n"
    "v_exp_f32 v44, v44\n"
    "v_exp_f32 v45, v45\n"
    "v_mfma_f32_32x32x4_2b_bf16 v[72:103], v[118:119], v[112:113], 0\n"
    "v_exp_f32 v46, v46\n"
    "v_exp_f32 v47, v47\n"
    "v_exp_f32 v48, v48\n"
    "v_exp_f32 v49, v49\n"
    "v_exp_f32 v50, v50\n"
    "v_exp_f32 v51, v51\n"
    "v_exp_f32 v52, v52\n"
    "v_exp_f32 v53, v53\n"
    "v_smfmac_f32_32x32x32_bf16 v[8:23], v[120:123], v[40:47], v128\n"
    "s_waitcnt lgkmcnt(0)\n"
    "ds_read_b64_tr_b16 v[118:119], v130 offset:9344\n"
    "v_exp_f32 v54, v54\n"
    "v_exp_f32 v55, v55\n"
    "v_exp_f32 v56, v56\n"
    "v_exp_f32 v57, v57\n"
    "v_smfmac_f32_32x32x32_bf16 v[8:23], v[124:127], v[48:55], v128\n"
    "v_exp_f32 v58, v58\n"
    "v_exp_f32 v59, v59\n"
    "v_exp_f32 v60, v60\n"
    "v_exp_f32 v61, v61\n"
    "v_mfma_f32_32x32x16_bf16 v[72:87], v[114:117], v[104:107], v[72:87]\n"
    "v_exp_f32 v62, v62\n"
    "v_exp_f32 v63, v63\n"
    "v_exp_f32 v64, v64\n"
    "v_exp_f32 v65, v65\n"
    "v_smfmac_f32_32x32x32_bf16 v[24:39], v[120:123], v[56:63], v128\n"
    "ds_read_b128 v[120:123], v131 offset:7008\n"
    "v_exp_f32 v66, v66\n"
    "v_exp_f32 v67, v67\n"
    "v_exp_f32 v68, v68\n"
    "v_mfma_f32_32x32x16_bf16 v[88:103], v[114:117], v[108:111], v[88:103]\n"
    "ds_read_b64_tr_b16 v[114:115], v129 offset:9344\n"
    "ds_read_b64_tr_b16 v[116:117], v129 offset:9408\n"
    "v_exp_f32 v69, v69\n"
    "v_exp_f32 v70, v70\n"
    "v_exp_f32 v71, v71\n"
    "v_exp_f32 v72, v72\n"
    "v_exp_f32 v73, v73\n"
    "v_smfmac_f32_32x32x32_bf16 v[24:39], v[124:127], v[64:71], v128\n"
    "s_waitcnt lgkmcnt(2)\n"
    "ds_read_b128 v[124:127], v132 offset:7008\n"
    "s_waitcnt vmcnt(2)\n"
    "ds_read_b128 v[136:139], v2 offset:0\n"
    "ds_read_b128 v[140:143], v2 offset:1024\n"
    "v_exp_f32 v74, v74\n"
    "v_exp_f32 v75, v75\n"
    "v_exp_f32 v76, v76\n"
    "v_exp_f32 v77, v77\n"
    "v_mfma_f32_32x32x4_2b_bf16 v[40:71], v[118:119], v[112:113], 0\n"
    "v_exp_f32 v78, v78\n"
    "v_exp_f32 v79, v79\n"
    "v_exp_f32 v80, v80\n"
    "v_exp_f32 v81, v81\n"
    "v_exp_f32 v82, v82\n"
    "v_exp_f32 v83, v83\n"
    "v_exp_f32 v84, v84\n"
    "v_exp_f32 v85, v85\n"
    "v_smfmac_f32_32x32x32_bf16 v[8:23], v[120:123], v[72:79], v128\n"
    "s_waitcnt lgkmcnt(0)\n"
    "ds_read_b64_tr_b16 v[118:119], v130 offset:11680\n"
    "s_cmp_eq_u32 %[fh], 0\n"
    "s_cbranch_scc1 8f\n"
    "v_mov_b32 v252, s46\n"
    "v_mov_b32 v253, s47\n"
    "v_mov_b32 v254, %[fh]\n"
    "s_waitcnt vmcnt(4)\n"
    "s_mov_b64 exec, 1\n"
    "global_store_dwordx2 v254, v[252:253], %[ws] sc1\n"
    "s_mov_b64 exec, -1\n"
    "8:\n"
    "v_exp_f32 v86, v86\n"
    "v_exp_f32 v87, v87\n"
    "v_exp_f32 v88, v88\n"
    "v_exp_f32 v89, v89\n"
    "v_smfmac_f32_32x32x32_bf16 v[8:23], v[124:127], v[80:87], v128\n"
    "s_add_u32 m0, %[lc], 0\n"
    "s_nop 0\n"
    "global_load_lds_dwordx4 v244, s[48:49]\n"
    "v_exp_f32 v90, v90\n"
    "v_exp_f32 v91, v91\n"
    "v_exp_f32 v92, v92\n"
    "v_exp_f32 v93, v93\n"
    "v_mfma_f32_32x32x16_bf16 v[40:55], v[114:117], v[104:107], v[40:55]\n"
    "v_exp_f32 v94, v94\n"
    "v_exp_f32 v95, v95\n"
    "v_exp_f32 v96, v96\n"
    "v_exp_f32 v97, v97\n"
    "v_smfmac_f32_32x32x32_bf16 v[24:39], v[120:123], v[88:95], v128\n"
    "ds_read_b128 v[120:123], v131 offset:9344\n"
    "global_load_lds_dwordx4 v3, s[48:49] offset:1024\n"
    "s_add_u32 s48, s48, 64\n"
    "s_addc_u32 s49, s49, 0\n"
    "v_exp_f32 v98, v98\n"
    "v_exp_f32 v99, v99\n"
    "v_exp_f32 v100, v100\n"
    "v_mfma_f32_32x32x16_bf16 v[56:71], v[114:117], v[108:111], v[56:71]\n"
    "ds_read_b64_tr_b16 v[114:115], v129 offset:11680\n"
    "ds_read_b64_tr_b16 v[116:117], v129 offset:11744\n"
    "v_exp_f32 v101, v101\n"
    "v_exp_f32 v102, v102\n"
    "v_exp_f32 v103, v103\n"
    "v_exp_f32 v40, v40\n"
    "v_exp_f32 v41, v41\n"
    "v_smfmac_f32_32x32x32_bf16 v[24:39], v[124:127], v[96:103], v128\n"
    "s_waitcnt lgkmcnt(2)\n"
    "ds_read_b128 v[124:127], v132 offset:9344\n"
    "s_waitcnt vmcnt(2)\n"
    "ds_read_b128 v[144:147], v2 offset:2048\n"
    "ds_read_b128 v[148:151], v2 offset:3072\n"
    "v_exp_f32 v42, v42\n"
    "v_exp_f32 v43, v43\n"
    "v_exp_f32 v44, v44\n"
    "v_exp_f32 v45, v45\n"
    "v_mfma_f32_32x32x4_2b_bf16 v[72:103], v[118:119], v[112:113], 0\n"
    "v_exp_f32 v46, v46\n"
    "v_exp_f32 v47, v47\n"
    "v_exp_f32 v48, v48\n"
    "v_exp_f32 v49, v49\n"
    "v_exp_f32 v50, v50\n"
    "v_exp_f32 v51, v51\n"
    "v_exp_f32 v52, v52\n"
    "v_exp_f32 v53, v53\n"
    "v_smfmac_f32_32x32x32_bf16 v[8:23], v[120:123], v[40:47], v128\n"
    "s_waitcnt lgkmcnt(0)\n"
    "ds_read_b64_tr_b16 v[118:119], v130 offset:14016\n"
    "v_exp_f32 v54, v54\n"
    "v_exp_f32 v55, v55\n"
    "v_exp_f32 v56, v56\n"
    "v_exp_f32 v57, v57\n"
    "v_smfmac_f32_32x32x32_bf16 v[8:23], v[124:127], v[48:55], v128\n"
    "s_add_u32 m0, %[lc], 2048\n"
    "s_nop 0\n"
    "global_load_lds_dwordx4 v244, s[48:49]\n"
    "v_exp_f32 v58, v58\n"
    "v_exp_f32 v59, v59\n"
    "v_exp_f32 v60, v60\n"
    "v_exp_f32 v61, v61\n"
    "v_mfma_f32_32x32x16_bf16 v[72:87], v[114:117], v[104:107], v[72:87]\n"
    "v_exp_f32 v62, v62\n"
    "v_exp_f32 v63, v63\n"
    "v_exp_f32 v64, v64\n"
    "v_exp_f32 v65, v65\n"
    "v_smfmac_f32_32x32x32_bf16 v[24:39], v[120:123], v[56:63], v128\n"
    "ds_read_b128 v[120:123], v131 offset:11680\n"
    "global_load_lds_dwordx4 v3, s[48:49] offset:1024\n"
    "s_add_u32 s48, s48, 64\n"
    "s_addc_u32 s49, s49, 0\n"
    "v_exp_f32 v66, v66\n"
    "v_exp_f32 v67, v67\n"
    "v_exp_f32 v68, v68\n"
    "v_mfma_f32_32x32x16_bf16 v[88:103], v[114:117], v[108:111], v[88:103]\n"
    "ds_read_b64_tr_b16 v[114:115], v129 offset:14016\n"
    "ds_read_b64_tr_b16 v[116:117], v129 offset:14080\n"
    "v_exp_f32 v69, v69\n"
    "v_exp_f32 v70, v70\n"
    "v_exp_f32 v71, v71\n"
    "v_exp_f32 v72, v72\n"
    "v_exp_f32 v73, v73\n"
    "v_smfmac_f32_32x32x32_bf16 v[24:39], v[124:127], v[64:71], v128\n"
    "s_waitcnt lgkmcnt(2)\n"
    "ds_read_b128 v[124:127], v132 offset:11680\n"
    "v_exp_f32 v74, v74\n"
    "v_exp_f32 v75, v75\n"
    "v_exp_f32 v76, v76\n"
    "v_exp_f32 v77, v77\n"
    "v_mfma_f32_32x32x4_2b_bf16 v[40:71], v[118:119], v[112:113], 0\n"
    "v_exp_f32 v78, v78\n"
    "v_exp_f32 v79, v79\n"
    "v_exp_f32 v80, v80\n"
    "v_exp_f32 v81, v81\n"
    "v_exp_f32 v82, v82\n"
    "v_exp_f32 v83, v83\n"
    "v_exp_f32 v84, v84\n"
    "v_exp_f32 v85, v85\n"
    "v_smfmac_f32_32x32x32_bf16 v[8:23], v[120:123], v[72:79], v128\n"
    "s_waitcnt lgkmcnt(0)\n"
    "ds_read_b64_tr_b16 v[118:119], v130 offset:16352\n"
    "v_exp_f32 v86, v86\n"
    "v_exp_f32 v87, v87\n"
    "v_exp_f32 v88, v88\n"
    "v_exp_f32 v89, v89\n"
    "v_smfmac_f32_32x32x32_bf16 v[8:23], v[124:127], v[80:87], v128\n"
    "v_exp_f32 v90, v90\n"
    "v_exp_f32 v91, v91\n"
    "v_exp_f32 v92, v92\n"
    "v_exp_f32 v93, v93\n"
    "v_mfma_f32_32x32x16_bf16 v[40:55], v[114:117], v[104:107], v[40:55]\n"
    "v_exp_f32 v94, v94\n"
    "v_exp_f32 v95, v95\n"
    "v_exp_f32 v96, v96\n"
    "v_exp_f32 v97, v97\n"
    "v_smfmac_f32_32x32x32_bf16 v[24:39], v[120:123], v[88:95], v128\n"
    "ds_read_b128 v[120:123], v131 offset:14016\n"
    "v_exp_f32 v98, v98\n"
    "v_exp_f32 v99, v99\n"
    "v_exp_f32 v100, v100\n"
    "v_mfma_f32_32x32x16_bf16 v[56:71], v[114:117], v[108:111], v[56:71]\n"
    "ds_read_b64_tr_b16 v[114:115], v129 offset:16352\n"
    "ds_read_b64_tr_b16 v[116:117], v129 offset:16416\n"
    "v_exp_f32 v101, v101\n"
    "v_exp_f32 v102, v102\n"
    "v_exp_f32 v103, v103\n"
    "v_exp_f32 v40, v40\n"
    "v_exp_f32 v41, v41\n"
    "v_smfmac_f32_32x32x32_bf16 v[24:39], v[124:127], v[96:103], v128\n"
    "s_waitcnt lgkmcnt(2)\n"
    "ds_read_b128 v[124:127], v132 offset:14016\n"
    "s_waitcnt vmcnt(2)\n"
    "ds_read_b128 v[152:155], v2 offset:0\n"
    "ds_read_b128 v[156:159], v2 offset:1024\n"
    "v_exp_f32 v42, v42\n"
    "v_exp_f32 v43, v43\n"
    "v_exp_f32 v44, v44\n"
    "v_exp_f32 v45, v45\n"
    "v_mfma_f32_32x32x4_2b_bf16 v[72:103], v[118:119], v[112:113], 0\n"
    "v_exp_f32 v46, v46\n"
    "v_exp_f32 v47, v47\n"
    "v_exp_f32 v48, v48\n"
    "v_exp_f32 v49, v49\n"
    "v_exp_f32 v50, v50\n"
    "v_exp_f32 v51, v51\n"
    "v_exp_f32 v52, v52\n"
    "v_exp_f32 v53, v53\n"
    "v_smfmac_f32_32x32x32_bf16 v[8:23], v[120:123], v[40:47], v128\n"
    "s_waitcnt lgkmcnt(0)\n"
    "ds_read_b64_tr_b16 v[118:119], v130 offset:18688\n"
    "v_exp_f32 v54, v54\n"
    "v_exp_f32 v55, v55\n"
    "v_exp_f32 v56, v56\n"
    "v_exp_f32 v57, v57\n"
    "v_smfmac_f32_32x32x32_bf16 v[8:23], v[124:127], v[48:55], v128\n"
    "s_add_u32 m0, %[lc], 0\n"
    "s_nop 0\n"
    "global_load_lds_dwordx4 v244, s[48:49]\n"
    "v_exp_f32 v58, v58\n"
    "v_exp_f32 v59, v59\n"
    "v_exp_f32 v60, v60\n"
    "v_exp_f32 v61, v61\n"
    "v_mfma_f32_32x32x16_bf16 v[72:87], v[114:117], v[104:107], v[72:87]\n"
    "v_exp_f32 v62, v62\n"
    "v_exp_f32 v63, v63\n"
    "v_exp_f32 v64, v64\n"
    "v_exp_f32 v65, v65\n"
    "v_smfmac_f32_32x32x32_bf16 v[24:39], v[120:123], v[56:63], v128\n"
    "ds_read_b128 v[120:123], v131 offset:16352\n"
    "global_load_lds_dwordx4 v3, s[48:49] offset:1024\n"
    "s_add_u32 s48, s48, 64\n"
    "s_addc_u32 s49, s49, 0\n"
    "v_exp_f32 v66, v66\n"
    "v_exp_f32 v67, v67\n"
    "v_exp_f32 v68, v68\n"
    "v_mfma_f32_32x32x16_bf16 v[88:103], v[114:117], v[108:111], v[88:103]\n"
    "ds_read_b64_tr_b16 v[114:115], v129 offset:18688\n"
    "ds_read_b64_tr_b16 v[116:117], v129 offset:18752\n"
    "v_exp_f32 v69, v69\n"
    "v_exp_f32 v70, v70\n"
    "v_exp_f32 v71, v71\n"
    "v_exp_f32 v72, v72\n"
    "v_exp_f32 v73, v73\n"
    "v_smfmac_f32_32x32x32_bf16 v[24:39], v[124:127], v[64:71], v128\n"
    "s_waitcnt lgkmcnt(2)\n"
    "ds_read_b128 v[124:127], v132 offset:16352\n"
    "s_waitcnt vmcnt(2)\n"
    "ds_read_b128 v[160:163], v2 offset:2048\n"
    "ds_read_b128 v[164:167], v2 offset:3072\n"
    "v_exp_f32 v74, v74\n"
    "v_exp_f32 v75, v75\n"
    "v_exp_f32 v76, v76\n"
    "v_exp_f32 v77, v77\n"
    "v_mfma_f32_32x32x4_2b_bf16 v[40:71], v[118:119], v[112:113], 0\n"
    "v_exp_f32 v78, v78\n"
    "v_exp_f32 v79, v79\n"
    "v_exp_f32 v80, v80\n"
    "v_exp_f32 v81, v81\n"
    "v_exp_f32 v82, v82\n"
    "v_exp_f32 v83, v83\n"
    "v_exp_f32 v84, v84\n"
    "v_exp_f32 v85, v85\n"
    "v_smfmac_f32_32x32x32_bf16 v[8:23], v[120:123], v[72:79], v128\n"
    "s_waitcnt lgkmcnt(0)\n"
    "ds_read_b64_tr_b16 v[118:119], v130 offset:21024\n"
    "v_exp_f32 v86, v86\n"
    "v_exp_f32 v87, v87\n"
    "v_exp_f32 v88, v88\n"
    "v_exp_f32 v89, v89\n"
    "v_smfmac_f32_32x32x32_bf16 v[8:23], v[124:127], v[80:87], v128\n"
    "s_add_u32 m0, %[lc], 2048\n"
    "s_nop 0\n"
    "global_load_lds_dwordx4 v244, s[48:49]\n"
    "v_exp_f32 v90, v90\n"
    "v_exp_f32 v91, v91\n"
    "v_exp_f32 v92, v92\n"
    "v_exp_f32 v93, v93\n"
    "v_mfma_f32_32x32x16_bf16 v[40:55], v[114:117], v[104:107], v[40:55]\n"
    "v_exp_f32 v94, v94\n"
    "v_exp_f32 v95, v95\n"
    "v_exp_f32 v96, v96\n"
    "v_exp_f32 v97, v97\n"
    "v_smfmac_f32_32x32x32_bf16 v[24:39], v[120:123], v[88:95], v128\n"
    "ds_read_b128 v[120:123], v131 offset:18688\n"
    "global_load_lds_dwordx4 v3, s[48:49] offset:1024\n"
    "s_add_u32 s48, s48, 64\n"
    "s_addc_u32 s49, s49, 0\n"
    "v_exp_f32 v98, v98\n"
    "v_exp_f32 v99, v99\n"
    "v_exp_f32 v100, v100\n"
    "v_mfma_f32_32x32x16_bf16 v[56:71], v[114:117], v[108:111], v[56:71]\n"
    "ds_read_b64_tr_b16 v[114:115], v129 offset:21024\n"
    "ds_read_b64_tr_b16 v[116:117], v129 offset:21088\n"
    "v_exp_f32 v101, v101\n"
    "v_exp_f32 v102, v102\n"
    "v_exp_f32 v103, v103\n"
    "v_exp_f32 v40, v40\n"
    "v_exp_f32 v41, v41\n"
    "v_smfmac_f32_32x32x32_bf16 v[24:39], v[124:127], v[96:103], v128\n"
    "s_waitcnt lgkmcnt(2)\n"
    "ds_read_b128 v[124:127], v132 offset:18688\n"
    "v_exp_f32 v42, v42\n"
    "v_exp_f32 v43, v43\n"
    "v_exp_f32 v44, v44\n"
    "v_exp_f32 v45, v45\n"
    "v_mfma_f32_32x32x4_2b_bf16 v[72:103], v[118:119], v[112:113], 0\n"
    "v_exp_f32 v46, v46\n"
    "v_exp_f32 v47, v47\n"
    "v_exp_f32 v48, v48\n"
    "v_exp_f32 v49, v49\n"
    "v_exp_f32 v50, v50\n"
    "v_exp_f32 v51, v51\n"
    "v_exp_f32 v52, v52\n"
    "v_exp_f32 v53, v53\n"
    "v_smfmac_f32_32x32x32_bf16 v[8:23], v[120:123], v[40:47], v128\n"
    "s_waitcnt lgkmcnt(0)\n"
    "ds_read_b64_tr_b16 v[118:119], v130 offset:23360\n"
    "v_exp_f32 v54, v54\n"
    "v_exp_f32 v55, v55\n"
    "v_exp_f32 v56, v56\n"
    "v_exp_f32 v57, v57\n"
    "v_smfmac_f32_32x32x32_bf16 v[8:23], v[124:127], v[48:55], v128\n"
    "v_exp_f32 v58, v58\n"
    "v_exp_f32 v59, v59\n"
    "v_exp_f32 v60, v60\n"
    "v_exp_f32 v61, v61\n"
    "v_mfma_f32_32x32x16_bf16 v[72:87], v[114:117], v[104:107], v[72:87]\n"
    "v_exp_f32 v62, v62\n"
    "v_exp_f32 v63, v63\n"
    "v_exp_f32 v64, v64\n"
    "v_exp_f32 v65, v65\n"
    "v_smfmac_f32_32x32x32_bf16 v[24:39], v[120:123], v[56:63], v128\n"
    "ds_read_b128 v[120:123], v131 offset:21024\n"
    "v_exp_f32 v66, v66\n"
    "v_exp_f32 v67, v67\n"
    "v_exp_f32 v68, v68\n"
    "v_mfma_f32_32x32x16_bf16 v[88:103], v[114:117], v[108:111], v[88:103]\n"
    "ds_read_b64_tr_b16 v[114:115], v129 offset:23360\n"
    "ds_read_b64_tr_b16 v[116:117], v129 offset:23424\n"
    "v_exp_f32 v69, v69\n"
    "v_exp_f32 v70, v70\n"
    "v_exp_f32 v71, v71\n"
    "v_exp_f32 v72, v72\n"
    "v_exp_f32 v73, v73\n"
    "v_smfmac_f32_32x32x32_bf16 v[24:39], v[124:127], v[64:71], v128\n"
    "s_waitcnt lgkmcnt(2)\n"
    "ds_read_b128 v[124:127], v132 offset:21024\n"
    "s_setprio 1\n"
    "s_waitcnt vmcnt(2)\n"
    "ds_read_b128 v[168:171], v2 offset:0\n"
    "ds_read_b128 v[172:175], v2 offset:1024\n"
    "v_exp_f32 v74, v74\n"
    "v_exp_f32 v75, v75\n"
    "v_exp_f32 v76, v76\n"
    "v_exp_f32 v77, v77\n"
    "v_mfma_f32_32x32x4_2b_bf16 v[40:71], v[118:119], v[112:113], 0\n"
    "v_exp_f32 v78, v78\n"
    "v_exp_f32 v79, v79\n"
    "v_exp_f32 v80, v80\n"
    "v_exp_f32 v81, v81\n"
    "v_exp_f32 v82, v82\n"
    "v_exp_f32 v83, v83\n"
    "v_exp_f32 v84, v84\n"
    "v_exp_f32 v85, v85\n"
    "v_smfmac_f32_32x32x32_bf16 v[8:23], v[120:123], v[72:79], v128\n"
    "s_waitcnt lgkmcnt(0)\n"
    "ds_read_b64_tr_b16 v[118:119], v130 offset:25696\n"
    "v_exp_f32 v86, v86\n"
    "v_exp_f32 v87, v87\n"
    "v_exp_f32 v88, v88\n"
    "v_exp_f32 v89, v89\n"
    "v_smfmac_f32_32x32x32_bf16 v[8:23], v[124:127], v[80:87], v128\n"
    "s_add_u32 m0, %[lc], 0\n"
    "s_nop 0\n"
    "global_load_lds_dwordx4 v244, s[48:49]\n"
    "v_exp_f32 v90, v90\n"
    "v_exp_f32 v91, v91\n"
    "v_exp_f32 v92, v92\n"
    "v_exp_f32 v93, v93\n"
    "v_mfma_f32_32x32x16_bf16 v[40:55], v[114:117], v[104:107], v[40:55]\n"
    "v_exp_f32 v94, v94\n"
    "v_exp_f32 v95, v95\n"
    "v_exp_f32 v96, v96\n"
    "v_exp_f32 v97, v97\n"
    "v_smfmac_f32_32x32x32_bf16 v[24:39], v[120:123], v[88:95], v128\n"
    "ds_read_b128 v[120:123], v131 offset:23360\n"
    "global_load_lds_dwordx4 v3, s[48:49] offset:1024\n"
    "s_add_u32 s48, s48, 64\n"
    "s_addc_u32 s49, s49, 0\n"
    "v_exp_f32 v98, v98\n"
    "v_exp_f32 v99, v99\n"
    "v_exp_f32 v100, v100\n"
    "v_mfma_f32_32x32x16_bf16 v[56:71], v[114:117], v[108:111], v[56:71]\n"
    "ds_read_b64_tr_b16 v[114:115], v129 offset:25696\n"
    "ds_read_b64_tr_b16 v[116:117], v129 offset:25760\n"
    "v_exp_f32 v101, v101\n"
    "v_exp_f32 v102, v102\n"
    "v_exp_f32 v103, v103\n"
    "v_exp_f32 v40, v40\n"
    "v_exp_f32 v41, v41\n"
    "v_smfmac_f32_32x32x32_bf16 v[24:39], v[124:127], v[96:103], v128\n"
    "s_waitcnt lgkmcnt(2)\n"
    "ds_read_b128 v[124:127], v132 offset:23360\n"
    "s_waitcnt vmcnt(2)\n"
    "ds_read_b128 v[176:179], v2 offset:2048\n"
    "ds_read_b128 v[180:183], v2 offset:3072\n"
    "v_exp_f32 v42, v42\n"
    "v_exp_f32 v43, v43\n"
    "v_exp_f32 v44, v44\n"
    "v_exp_f32 v45, v45\n"
    "v_mfma_f32_32x32x4_2b_bf16 v[72:103], v[118:119], v[112:113], 0\n"
    "v_exp_f32 v46, v46\n"
    "v_exp_f32 v47, v47\n"
    "v_exp_f32 v48, v48\n"
    "v_exp_f32 v49, v49\n"
    "v_exp_f32 v50, v50\n"
    "v_exp_f32 v51, v51\n"
    "v_exp_f32 v52, v52\n"
    "v_exp_f32 v53, v53\n"
    "v_smfmac_f32_32x32x32_bf16 v[8:23], v[120:123], v[40:47], v128\n"
    "s_waitcnt lgkmcnt(0)\n"
    "ds_read_b64_tr_b16 v[118:119], v130 offset:28032\n"
    "v_exp_f32 v54, v54\n"
    "v_exp_f32 v55, v55\n"
    "v_exp_f32 v56, v56\n"
    "v_exp_f32 v57, v57\n"
    "v_smfmac_f32_32x32x32_bf16 v[8:23], v[124:127], v[48:55], v128\n"
    "s_add_u32 m0, %[lc], 2048\n"
    "s_nop 0\n"
    "global_load_lds_dwordx4 v244, s[48:49]\n"
    "v_exp_f32 v58, v58\n"
    "v_exp_f32 v59, v59\n"
    "v_exp_f32 v60, v60\n"
    "v_exp_f32 v61, v61\n"
    "v_mfma_f32_32x32x16_bf16 v[72:87], v[114:117], v[104:107], v[72:87]\n"
    "v_exp_f32 v62, v62\n"
    "v_exp_f32 v63, v63\n"
    "v_exp_f32 v64, v64\n"
    "v_exp_f32 v65, v65\n"
    "v_smfmac_f32_32x32x32_bf16 v[24:39], v[120:123], v[56:63], v128\n"
    "ds_read_b128 v[120:123], v131 offset:25696\n"
    "global_load_lds_dwordx4 v3, s[48:49] offset:1024\n"
    "s_add_u32 s48, s48, 64\n"
    "s_addc_u32 s49, s49, 0\n"
    "v_exp_f32 v66, v66\n"
    "v_exp_f32 v67, v67\n"
    "v_exp_f32 v68, v68\n"
    "v_mfma_f32_32x32x16_bf16 v[88:103], v[114:117], v[108:111], v[88:103]\n"
    "ds_read_b64_tr_b16 v[114:115], v129 offset:28032\n"
    "ds_read_b64_tr_b16 v[116:117], v129 offset:28096\n"
    "v_exp_f32 v69, v69\n"
    "v_exp_f32 v70, v70\n"
    "v_exp_f32 v71, v71\n"
    "v_exp_f32 v72, v72\n"
    "v_exp_f32 v73, v73\n"
    "v_smfmac_f32_32x32x32_bf16 v[24:39], v[124:127], v[64:71], v128\n"
    "s_waitcnt lgkmcnt(2)\n"
    "ds_read_b128 v[124:127], v132 offset:25696\n"
    "v_exp_f32 v74, v74\n"
    "v_exp_f32 v75, v75\n"
    "v_exp_f32 v76, v76\n"
    "v_exp_f32 v77, v77\n"
    "v_mfma_f32_32x32x4_2b_bf16 v[40:71], v[118:119], v[112:113], 0\n"
    "v_exp_f32 v78, v78\n"
    "v_exp_f32 v79, v79\n"
    "v_exp_f32 v80, v80\n"
    "v_exp_f32 v81, v81\n"
    "v_exp_f32 v82, v82\n"
    "v_exp_f32 v83, v83\n"
    "v_exp_f32 v84, v84\n"
    "v_exp_f32 v85, v85\n"
    "v_smfmac_f32_32x32x32_bf16 v[8:23], v[120:123], v[72:79], v128\n"
    "s_waitcnt lgkmcnt(0)\n"
    "ds_read_b64_tr_b16 v[118:119], v130 offset:30368\n"
    "v_exp_f32 v86, v86\n"
    "v_exp_f32 v87, v87\n"
    "v_exp_f32 v88, v88\n"
    "v_exp_f32 v89, v89\n"
    "v_smfmac_f32_32x32x32_bf16 v[8:23], v[124:127], v[80:87], v128\n"
    "v_exp_f32 v90, v90\n"
    "v_exp_f32 v91, v91\n"
    "v_exp_f32 v92, v92\n"
    "v_exp_f32 v93, v93\n"
    "v_mfma_f32_32x32x16_bf16 v[40:55], v[114:117], v[104:107], v[40:55]\n"
    "v_exp_f32 v94, v94\n"
    "v_exp_f32 v95, v95\n"
    "v_exp_f32 v96, v96\n"
    "v_exp_f32 v97, v97\n"
    "v_smfmac_f32_32x32x32_bf16 v[24:39], v[120:123], v[88:95], v128\n"
    "ds_read_b128 v[120:123], v131 offset:28032\n"
    "v_exp_f32 v98, v98\n"
    "v_exp_f32 v99, v99\n"
    "v_exp_f32 v100, v100\n"
    "v_mfma_f32_32x32x16_bf16 v[56:71], v[114:117], v[108:111], v[56:71]\n"
    "ds_read_b64_tr_b16 v[114:115], v129 offset:30368\n"
    "ds_read_b64_tr_b16 v[116:117], v129 offset:30432\n"
    "v_exp_f32 v101, v101\n"
    "v_exp_f32 v102, v102\n"
    "v_exp_f32 v103, v103\n"
    "v_exp_f32 v40, v40\n"
    "v_exp_f32 v41, v41\n"
    "v_smfmac_f32_32x32x32_bf16 v[24:39], v[124:127], v[96:103], v128\n"
    "s_waitcnt lgkmcnt(2)\n"
    "ds_read_b128 v[124:127], v132 offset:28032\n"
    "s_waitcnt vmcnt(2)\n"
    "ds_read_b128 v[184:187], v2 offset:0\n"
    "ds_read_b128 v[188:191], v2 offset:1024\n"
    "v_exp_f32 v42, v42\n"
    "v_exp_f32 v43, v43\n"
    "v_exp_f32 v44, v44\n"
    "v_exp_f32 v45, v45\n"
    "v_mfma_f32_32x32x4_2b_bf16 v[72:103], v[118:119], v[112:113], 0\n"
    "v_exp_f32 v46, v46\n"
    "v_exp_f32 v47, v47\n"
    "v_exp_f32 v48, v48\n"
    "v_exp_f32 v49, v49\n"
    "v_exp_f32 v50, v50\n"
    "v_exp_f32 v51, v51\n"
    "v_exp_f32 v52, v52\n"
    "v_exp_f32 v53, v53\n"
    "v_smfmac_f32_32x32x32_bf16 v[8:23], v[120:123], v[40:47], v128\n"
    "s_waitcnt lgkmcnt(0)\n"
    "ds_read_b64_tr_b16 v[118:119], v130 offset:32704\n"
    "v_exp_f32 v54, v54\n"
    "v_exp_f32 v55, v55\n"
    "v_exp_f32 v56, v56\n"
    "v_exp_f32 v57, v57\n"
    "v_smfmac_f32_32x32x32_bf16 v[8:23], v[124:127], v[48:55], v128\n"
    "s_add_u32 m0, %[lc], 0\n"
    "s_nop 0\n"
    "global_load_lds_dwordx4 v244, s[48:49]\n"
    "v_exp_f32 v58, v58\n"
    "v_exp_f32 v59, v59\n"
    "v_exp_f32 v60, v60\n"
    "v_exp_f32 v61, v61\n"
    "v_mfma_f32_32x32x16_bf16 v[72:87], v[114:117], v[104:107], v[72:87]\n"
    "v_exp_f32 v62, v62\n"
    "v_exp_f32 v63, v63\n"
    "v_exp_f32 v64, v64\n"
    "v_exp_f32 v65, v65\n"
    "v_smfmac_f32_32x32x32_bf16 v[24:39], v[120:123], v[56:63], v128\n"
    "ds_read_b128 v[120:123], v131 offset:30368\n"
    "global_load_lds_dwordx4 v3, s[48:49] offset:1024\n"
    "s_add_u32 s48, s48, 64\n"
    "s_addc_u32 s49, s49, 0\n"
    "v_exp_f32 v66, v66\n"
    "v_exp_f32 v67, v67\n"
    "v_exp_f32 v68, v68\n"
    "v_mfma_f32_32x32x16_bf16 v[88:103], v[114:117], v[108:111], v[88:103]\n"
    "ds_read_b64_tr_b16 v[114:115], v129 offset:32704\n"
    "ds_read_b64_tr_b16 v[116:117], v129 offset:32768\n"
    "v_exp_f32 v69, v69\n"
    "v_exp_f32 v70, v70\n"
    "v_exp_f32 v71, v71\n"
    "v_exp_f32 v72, v72\n"
    "v_exp_f32 v73, v73\n"
    "v_smfmac_f32_32x32x32_bf16 v[24:39], v[124:127], v[64:71], v128\n"
    "s_waitcnt lgkmcnt(2)\n"
    "ds_read_b128 v[124:127], v132 offset:30368\n"
    "s_setprio 0\n"
    "s_waitcnt vmcnt(2)\n"
    "ds_read_b128 v[192:195], v2 offset:2048\n"
    "ds_read_b128 v[196:199], v2 offset:3072\n"
    "v_exp_f32 v74, v74\n"
    "v_exp_f32 v75, v75\n"
    "v_exp_f32 v76, v76\n"
    "v_exp_f32 v77, v77\n"
    "v_mfma_f32_32x32x4_2b_bf16 v[40:71], v[118:119], v[112:113], 0\n"
    "v_exp_f32 v78, v78\n"
    "v_exp_f32 v79, v79\n"
    "v_exp_f32 v80, v80\n"
    "v_exp_f32 v81, v81\n"
    "v_exp_f32 v82, v82\n"
    "v_exp_f32 v83, v83\n"
    "v_exp_f32 v84, v84\n"
    "v_exp_f32 v85, v85\n"
    "v_smfmac_f32_32x32x32_bf16 v[8:23], v[120:123], v[72:79], v128\n"
    "s_waitcnt lgkmcnt(0)\n"
    "ds_read_b64_tr_b16 v[118:119], v130 offset:35040\n"
    "v_exp_f32 v86, v86\n"
    "v_exp_f32 v87, v87\n"
    "v_exp_f32 v88, v88\n"
    "v_exp_f32 v89, v89\n"
    "v_smfmac_f32_32x32x32_bf16 v[8:23], v[124:127], v[80:87], v128\n"
    "v_exp_f32 v90, v90\n"
    "v_exp_f32 v91, v91\n"
    "v_exp_f32 v92, v92\n"
    "v_exp_f32 v93, v93\n"
    "v_mfma_f32_32x32x16_bf16 v[40:55], v[114:117], v[104:107], v[40:55]\n"
    "v_exp_f32 v94, v94\n"
    "v_exp_f32 v95, v95\n"
    "v_exp_f32 v96, v96\n"
    "v_exp_f32 v97, v97\n"
    "v_smfmac_f32_32x32x32_bf16 v[24:39], v[120:123], v[88:95], v128\n"
    "ds_read_b128 v[120:123], v131 offset:32704\n"
    "v_exp_f32 v98, v98\n"
    "v_exp_f32 v99, v99\n"
    "v_exp_f32 v100, v100\n"
    "v_mfma_f32_32x32x16_bf16 v[56:71], v[114:117], v[108:111], v[56:71]\n"
    "ds_read_b64_tr_b16 v[114:115], v129 offset:35040\n"
    "ds_read_b64_tr_b16 v[116:117], v129 offset:35104\n"
    "v_exp_f32 v101, v101\n"
    "v_exp_f32 v102, v102\n"
    "v_exp_f32 v103, v103\n"
    "v_exp_f32 v40, v40\n"
    "v_exp_f32 v41, v41\n"
    "v_smfmac_f32_32x32x32_bf16 v[24:39], v[124:127], v[96:103], v128\n"
    "s_waitcnt lgkmcnt(2)\n"
    "ds_read_b128 v[124:127], v132 offset:32704\n"
    "global_load_dwordx2 v[252:253], v245, %[ws] sc1\n"
    "v_exp_f32 v42, v42\n"
    "v_exp_f32 v43, v43\n"
    "v_exp_f32 v44, v44\n"
    "v_exp_f32 v45, v45\n"
    "v_mfma_f32_32x32x4_2b_bf16 v[72:103], v[118:119], v[112:113], 0\n"
    "v_exp_f32 v46, v46\n"
    "v_exp_f32 v47, v47\n"
    "v_exp_f32 v48, v48\n"
    "v_exp_f32 v49, v49\n"
    "v_exp_f32 v50, v50\n"
    "v_exp_f32 v51, v51\n"
    "v_exp_f32 v52, v52\n"
    "v_exp_f32 v53, v53\n"
    "v_smfmac_f32_32x32x32_bf16 v[8:23], v[120:123], v[40:47], v128\n"
    "s_waitcnt lgkmcnt(0)\n"
    "ds_read_b64_tr_b16 v[118:119], v130 offset:37376\n"
    "v_exp_f32 v54, v54\n"
    "v_exp_f32 v55, v55\n"
    "v_exp_f32 v56, v56\n"
    "v_exp_f32 v57, v57\n"
    "v_smfmac_f32_32x32x32_bf16 v[8:23], v[124:127], v[48:55], v128\n"
    "v_exp_f32 v58, v58\n"
    "v_exp_f32 v59, v59\n"
    "v_exp_f32 v60, v60\n"
    "v_exp_f32 v61, v61\n"
    "v_mfma_f32_32x32x16_bf16 v[72:87], v[114:117], v[104:107], v[72:87]\n"
    "v_exp_f32 v62, v62\n"
    "v_exp_f32 v63, v63\n"
    "v_exp_f32 v64, v64\n"
    "v_exp_f32 v65, v65\n"
    "v_smfmac_f32_32x32x32_bf16 v[24:39], v[120:123], v[56:63], v128\n"
    "ds_read_b128 v[120:123], v131 offset:35040\n"
    "v_exp_f32 v66, v66\n"
    "v_exp_f32 v67, v67\n"
    "v_exp_f32 v68, v68\n"
    "v_mfma_f32_32x32x16_bf16 v[88:103], v[114:117], v[108:111], v[88:103]\n"
    "ds_read_b64_tr_b16 v[114:115], v129 offset:37376\n"
    "ds_read_b64_tr_b16 v[116:117], v129 offset:37440\n"
    "v_exp_f32 v69, v69\n"
    "v_exp_f32 v70, v70\n"
    "v_exp_f32 v71, v71\n"
    "v_exp_f32 v72, v72\n"
    "v_exp_f32 v73, v73\n"
    "v_smfmac_f32_32x32x32_bf16 v[24:39], v[124:127], v[64:71], v128\n"
    "s_waitcnt lgkmcnt(2)\n"
    "ds_read_b128 v[124:127], v132 offset:35040\n"
    "s_waitcnt vmcnt(1)\n"
    "ds_read_b128 v[200:203], v2 offset:0\n"
    "ds_read_b128 v[204:207], v2 offset:1024\n"
    "v_exp_f32 v74, v74\n"
    "v_exp_f32 v75, v75\n"
    "v_exp_f32 v76, v76\n"
    "v_exp_f32 v77, v77\n"
    "v_mfma_f32_32x32x4_2b_bf16 v[40:71], v[118:119], v[112:113], 0\n"
    "v_exp_f32 v78, v78\n"
    "v_exp_f32 v79, v79\n"
    "v_exp_f32 v80, v80\n"
    "v_exp_f32 v81, v81\n"
    "v_exp_f32 v82, v82\n"
    "v_exp_f32 v83, v83\n"
    "v_exp_f32 v84, v84\n"
    "v_exp_f32 v85, v85\n"
    "v_smfmac_f32_32x32x32_bf16 v[8:23], v[120:123], v[72:79], v128\n"
    "s_waitcnt lgkmcnt(0)\n"
    "ds_read_b64_tr_b16 v[118:119], v130 offset:39712\n"
    "v_exp_f32 v86, v86\n"
    "v_exp_f32 v87, v87\n"
    "v_exp_f32 v88, v88\n"
    "v_exp_f32 v89, v89\n"
    "v_smfmac_f32_32x32x32_bf16 v[8:23], v[124:127], v[80:87], v128\n"
    "s_waitcnt vmcnt(0)\n"
    "v_cmp_ne_u64 vcc, s[46:47], v[252:253]\n"
    "s_cbranch_vccz 5f\n"
    "s_mov_b32 %[cnt], 0x40000\n"
    "6:\n"
    "s_sleep 2\n"
    "global_load_dwordx2 v[252:253], v245, %[ws] sc1\n"
    "s_waitcnt vmcnt(0)\n"
    "v_cmp_ne_u64 vcc, s[46:47], v[252:253]\n"
    "s_sub_u32 %[cnt], %[cnt], 1\n"
    "s_cbranch_scc1 5f\n"
    "s_cbranch_vccnz 6b\n"
    "5:\n"
    "s_barrier\n"
    "v_exp_f32 v90, v90\n"
    "v_exp_f32 v91, v91\n"
    "v_exp_f32 v92, v92\n"
    "v_exp_f32 v93, v93\n"
    "v_mfma_f32_32x32x16_bf16 v[40:55], v[114:117], v[104:107], v[40:55]\n"
    "s_add_u32 m0, %[lq], 0\n"
    "s_nop 0\n"
    "global_load_lds_dwordx4 v133, s[44:45] offset:0 sc1\n"
    "v_exp_f32 v94, v94\n"
    "v_exp_f32 v95, v95\n"
    "v_exp_f32 v96, v96\n"
    "v_exp_f32 v97, v97\n"
    "v_smfmac_f32_32x32x32_bf16 v[24:39], v[120:123], v[88:95], v128\n"
    "ds_read_b128 v[120:123], v131 offset:37376\n"
    "global_load_lds_dwordx4 v133, s[44:45] offset:1024 sc1\n"
    "v_exp_f32 v98, v98\n"
    "v_exp_f32 v99, v99\n"
    "v_exp_f32 v100, v100\n"
    "v_mfma_f32_32x32x16_bf16 v[56:71], v[114:117], v[108:111], v[56:71]\n"
    "ds_read_b64_tr_b16 v[114:115], v129 offset:39712\n"
    "ds_read_b64_tr_b16 v[116:117], v129 offset:39776\n"
    "v_exp_f32 v101, v101\n"
    "v_exp_f32 v102, v102\n"
    "v_exp_f32 v103, v103\n"
    "v_exp_f32 v40, v40\n"
    "v_exp_f32 v41, v41\n"
    "v_smfmac_f32_32x32x32_bf16 v[24:39], v[124:127], v[96:103], v128\n"
    "s_waitcnt lgkmcnt(2)\n"
    "ds_read_b128 v[124:127], v132 offset:37376\n"
    "s_setprio 3\n"
    "v_exp_f32 v42, v42\n"
    "v_exp_f32 v43, v43\n"
    "v_exp_f32 v44, v44\n"
    "v_exp_f32 v45, v45\n"
    "v_mfma_f32_32x32x4_2b_bf16 v[72:103], v[118:119], v[112:113], 0\n"
    "v_exp_f32 v46, v46\n"
    "v_exp_f32 v47, v47\n"
    "v_exp_f32 v48, v48\n"
    "v_exp_f32 v49, v49\n"
    "v_exp_f32 v50, v50\n"
    "v_exp_f32 v51, v51\n"
    "v_exp_f32 v52, v52\n"
    "v_exp_f32 v53, v53\n"
    "v_smfmac_f32_32x32x32_bf16 v[8:23], v[120:123], v[40:47], v128\n"
    "s_waitcnt lgkmcnt(0)\n"
    "ds_read_b64_tr_b16 v[118:119], v130 offset:42048\n"
    "global_load_lds_dwordx4 v133, s[44:45] offset:2048 sc1\n"
    "v_exp_f32 v54, v54\n"
    "v_exp_f32 v55, v55\n"
    "v_exp_f32 v56, v56\n"
    "v_exp_f32 v57, v57\n"
    "v_smfmac_f32_32x32x32_bf16 v[8:23], v[124:127], v[48:55], v128\n"
    "global_load_lds_dwordx4 v133, s[44:45] offset:3072 sc1\n"
    "s_add_u32 s44, s44, 4096\n"
    "s_addc_u32 s45, s45, 0\n"
    "v_exp_f32 v58, v58\n"
    "v_exp_f32 v59, v59\n"
    "v_exp_f32 v60, v60\n"
    "v_exp_f32 v61, v61\n"
    "v_mfma_f32_32x32x16_bf16 v[72:87], v[114:117], v[104:107], v[72:87]\n"
    "s_add_u32 m0, %[lq], 4096\n"
    "s_nop 0\n"
    "global_load_lds_dwordx4 v133, s[44:45] offset:0 sc1\n"
    "s_add_u32 s44, s44, 32768\n"
    "s_addc_u32 s45, s45, 0\n"
    "v_exp_f32 v62, v62\n"
    "v_exp_f32 v63, v63\n"
    "v_exp_f32 v64, v64\n"
    "v_exp_f32 v65, v65\n"
    "v_smfmac_f32_32x32x32_bf16 v[24:39], v[120:123], v[56:63], v128\n"
    "ds_read_b128 v[120:123], v131 offset:39712\n"
    "s_add_u32 m0, %[lq], 18560\n"
    "s_nop 0\n"
    "global_load_lds_dwordx4 v133, s[44:45] offset:0 sc1\n"
    "v_exp_f32 v66, v66\n"
    "v_exp_f32 v67, v67\n"
    "v_exp_f32 v68, v68\n"
    "v_mfma_f32_32x32x16_bf16 v[88:103], v[114:117], v[108:111], v[88:103]\n"
    "ds_read_b64_tr_b16 v[114:115], v129 offset:42048\n"
    "ds_read_b64_tr_b16 v[116:117], v129 offset:42112\n"
    "v_exp_f32 v69, v69\n"
    "v_exp_f32 v70, v70\n"
    "v_exp_f32 v71, v71\n"
    "v_exp_f32 v72, v72\n"
    "v_exp_f32 v73, v73\n"
    "v_smfmac_f32_32x32x32_bf16 v[24:39], v[124:127], v[64:71], v128\n"
    "s_waitcnt lgkmcnt(2)\n"
    "ds_read_b128 v[124:127], v132 offset:39712\n"
    "v_exp_f32 v74, v74\n"
    "v_exp_f32 v75, v75\n"
    "v_exp_f32 v76, v76\n"
    "v_exp_f32 v77, v77\n"
    "v_mfma_f32_32x32x4_2b_bf16 v[40:71], v[118:119], v[112:113], 0\n"
    "v_exp_f32 v78, v78\n"
    "v_exp_f32 v79, v79\n"
    "v_exp_f32 v80, v80\n"
    "v_exp_f32 v81, v81\n"
    "v_exp_f32 v82, v82\n"
    "v_exp_f32 v83, v83\n"
    "v_exp_f32 v84, v84\n"
    "v_exp_f32 v85, v85\n"
    "v_smfmac_f32_32x32x32_bf16 v[8:23], v[120:123], v[72:79], v128\n"
    "s_waitcnt lgkmcnt(0)\n"
    "ds_read_b64_tr_b16 v[118:119], v130 offset:44384\n"
    "global_load_lds_dwordx4 v133, s[44:45] offset:1024 sc1\n"
    "v_exp_f32 v86, v86\n"
    "v_exp_f32 v87, v87\n"
    "v_exp_f32 v88, v88\n"
    "v_exp_f32 v89, v89\n"
    "v_smfmac_f32_32x32x32_bf16 v[8:23], v[124:127], v[80:87], v128\n"
    "global_load_lds_dwordx4 v133, s[44:45] offset:2048 sc1\n"
    "v_exp_f32 v90, v90\n"
    "v_exp_f32 v91, v91\n"
    "v_exp_f32 v92, v92\n"
    "v_exp_f32 v93, v93\n"
    "v_mfma_f32_32x32x16_bf16 v[40:55], v[114:117], v[104:107], v[40:55]\n"
    "global_load_lds_dwordx4 v133, s[44:45] offset:3072 sc1\n"
    "s_add_u32 s44, s44, 4096\n"
    "s_addc_u32 s45, s45, 0\n"
    "v_exp_f32 v94, v94\n"
    "v_exp_f32 v95, v95\n"
    "v_exp_f32 v96, v96\n"
    "v_exp_f32 v97, v97\n"
    "v_smfmac_f32_32x32x32_bf16 v[24:39], v[120:123], v[88:95], v128\n"
    "ds_read_b128 v[120:123], v131 offset:42048\n"
    "s_add_u32 m0, %[lq], 22656\n"
    "s_nop 0\n"
    "global_load_lds_dwordx4 v133, s[44:45] offset:0 sc1\n"
    "s_add_u32 s44, s44, 32768\n"
    "s_addc_u32 s45, s45, 0\n"
    "v_exp_f32 v98, v98\n"
    "v_exp_f32 v99, v99\n"
    "v_exp_f32 v100, v100\n"
    "v_mfma_f32_32x32x16_bf16 v[56:71], v[114:117], v[108:111], v[56:71]\n"
    "ds_read_b64_tr_b16 v[114:115], v129 offset:44384\n"
    "ds_read_b64_tr_b16 v[116:117], v129 offset:44448\n"
    "v_exp_f32 v101, v101\n"
    "v_exp_f32 v102, v102\n"
    "v_exp_f32 v103, v103\n"
    "v_exp_f32 v40, v40\n"
    "v_exp_f32 v41, v41\n"
    "v_smfmac_f32_32x32x32_bf16 v[24:39], v[124:127], v[96:103], v128\n"
    "s_waitcnt lgkmcnt(2)\n"
    "ds_read_b128 v[124:127], v132 offset:42048\n"
    "v_exp_f32 v42, v42\n"
    "v_exp_f32 v43, v43\n"
    "v_exp_f32 v44, v44\n"
    "v_exp_f32 v45, v45\n"
    "v_mfma_f32_32x32x4_2b_bf16 v[72:103], v[118:119], v[112:113], 0\n"
    "v_exp_f32 v46, v46\n"
    "v_exp_f32 v47, v47\n"
    "v_exp_f32 v48, v48\n"
    "v_exp_f32 v49, v49\n"
    "v_exp_f32 v50, v50\n"
    "v_exp_f32 v51, v51\n"
    "v_exp_f32 v52, v52\n"
    "v_exp_f32 v53, v53\n"
    "v_smfmac_f32_32x32x32_bf16 v[8:23], v[120:123], v[40:47], v128\n"
    "s_waitcnt lgkmcnt(0)\n"
    "ds_read_b64_tr_b16 v[118:119], v130 offset:46720\n"
    "s_add_u32 m0, %[lq], 37120\n"
    "s_nop 0\n"
    "global_load_lds_dwordx4 v133, s[44:45] offset:0 sc1\n"
    "v_exp_f32 v54, v54\n"
    "v_exp_f32 v55, v55\n"
    "v_exp_f32 v56, v56\n"
    "v_exp_f32 v57, v57\n"
    "v_smfmac_f32_32x32x32_bf16 v[8:23], v[124:127], v[48:55], v128\n"
    "global_load_lds_dwordx4 v133, s[44:45] offset:1024 sc1\n"
    "v_exp_f32 v58, v58\n"
    "v_exp_f32 v59, v59\n"
    "v_exp_f32 v60, v60\n"
    "v_exp_f32 v61, v61\n"
    "v_mfma_f32_32x32x16_bf16 v[72:87], v[114:117], v[104:107], v[72:87]\n"
    "global_load_lds_dwordx4 v133, s[44:45] offset:2048 sc1\n"
    "v_exp_f32 v62, v62\n"
    "v_exp_f32 v63, v63\n"
    "v_exp_f32 v64, v64\n"
    "v_exp_f32 v65, v65\n"
    "v_smfmac_f32_32x32x32_bf16 v[24:39], v[120:123], v[56:63], v128\n"
    "ds_read_b128 v[120:123], v131 offset:44384\n"
    "global_load_lds_dwordx4 v133, s[44:45] offset:3072 sc1\n"
    "s_add_u32 s44, s44, 4096\n"
    "s_addc_u32 s45, s45, 0\n"
    "v_exp_f32 v66, v66\n"
    "v_exp_f32 v67, v67\n"
    "v_exp_f32 v68, v68\n"
    "v_mfma_f32_32x32x16_bf16 v[88:103], v[114:117], v[108:111], v[88:103]\n"
    "ds_read_b64_tr_b16 v[114:115], v129 offset:46720\n"
    "ds_read_b64_tr_b16 v[116:117], v129 offset:46784\n"
    "v_exp_f32 v69, v69\n"
    "v_exp_f32 v70, v70\n"
    "v_exp_f32 v71, v71\n"
    "v_exp_f32 v72, v72\n"
    "v_exp_f32 v73, v73\n"
    "v_smfmac_f32_32x32x32_bf16 v[24:39], v[124:127], v[64:71], v128\n"
    "s_waitcnt lgkmcnt(2)\n"
    "ds_read_b128 v[124:127], v132 offset:44384\n"
    "s_setprio 2\n"
    "v_exp_f32 v74, v74\n"
    "v_exp_f32 v75, v75\n"
    "v_exp_f32 v76, v76\n"
    "v_exp_f32 v77, v77\n"
    "v_mfma_f32_32x32x4_2b_bf16 v[40:71], v[118:119], v[112:113], 0\n"
    "v_exp_f32 v78, v78\n"
    "v_exp_f32 v79, v79\n"
    "v_exp_f32 v80, v80\n"
    "v_exp_f32 v81, v81\n"
    "v_exp_f32 v82, v82\n"
    "v_exp_f32 v83, v83\n"
    "v_exp_f32 v84, v84\n"
    "v_exp_f32 v85, v85\n"
    "v_smfmac_f32_32x32x32_bf16 v[8:23], v[120:123], v[72:79], v128\n"
    "s_waitcnt lgkmcnt(0)\n"
    "ds_read_b64_tr_b16 v[118:119], v130 offset:49056\n"
    "s_add_u32 m0, %[lq], 41216\n"
    "s_nop 0\n"
    "global_load_lds_dwordx4 v133, s[44:45] offset:0 sc1\n"
    "s_add_u32 s44, s44, 32768\n"
    "s_addc_u32 s45, s45, 0\n"
    "v_exp_f32 v86, v86\n"
    "v_exp_f32 v87, v87\n"
    "v_exp_f32 v88, v88\n"
    "v_exp_f32 v89, v89\n"
    "v_smfmac_f32_32x32x32_bf16 v[8:23], v[124:127], v[80:87], v128\n"
    "global_load_dwordx4 v[208:211], v133, s[42:43] offset:0 sc1\n"
    "v_exp_f32 v90, v90\n"
    "v_exp_f32 v91, v91\n"
    "v_exp_f32 v92, v92\n"
    "v_exp_f32 v93, v93\n"
    "v_mfma_f32_32x32x16_bf16 v[40:55], v[114:117], v[104:107], v[40:55]\n"
    "global_load_dwordx4 v[216:219], v133, s[42:43] offset:1024 sc1\n"
    "v_exp_f32 v94, v94\n"
    "v_exp_f32 v95, v95\n"
    "v_exp_f32 v96, v96\n"
    "v_exp_f32 v97, v97\n"
    "v_smfmac_f32_32x32x32_bf16 v[24:39], v[120:123], v[88:95], v128\n"
    "ds_read_b128 v[120:123], v131 offset:46720\n"
    "global_load_dwordx4 v[224:227], v133, s[42:43] offset:2048 sc1\n"
    "v_exp_f32 v98, v98\n"
    "v_exp_f32 v99, v99\n"
    "v_exp_f32 v100, v100\n"
    "v_mfma_f32_32x32x16_bf16 v[56:71], v[114:117], v[108:111], v[56:71]\n"
    "ds_read_b64_tr_b16 v[114:115], v129 offset:49056\n"
    "ds_read_b64_tr_b16 v[116:117], v129 offset:49120\n"
    "v_exp_f32 v101, v101\n"
    "v_exp_f32 v102, v102\n"
    "v_exp_f32 v103, v103\n"
    "v_exp_f32 v40, v40\n"
    "v_exp_f32 v41, v41\n"
    "v_smfmac_f32_32x32x32_bf16 v[24:39], v[124:127], v[96:103], v128\n"
    "s_waitcnt lgkmcnt(2)\n"
    "ds_read_b128 v[124:127], v132 offset:46720\n"
    "v_exp_f32 v42, v42\n"
    "v_exp_f32 v43, v43\n"
    "v_exp_f32 v44, v44\n"
    "v_exp_f32 v45, v45\n"
    "v_mfma_f32_32x32x4_2b_bf16 v[72:103], v[118:119], v[112:113], 0\n"
    "v_exp_f32 v46, v46\n"
    "v_exp_f32 v47, v47\n"
    "v_exp_f32 v48, v48\n"
    "v_exp_f32 v49, v49\n"
    "v_exp_f32 v50, v50\n"
    "v_exp_f32 v51, v51\n"
    "v_exp_f32 v52, v52\n"
    "v_exp_f32 v53, v53\n"
    "v_smfmac_f32_32x32x32_bf16 v[8:23], v[120:123], v[40:47], v128\n"
    "s_waitcnt lgkmcnt(0)\n"
    "ds_read_b64_tr_b16 v[118:119], v130 offset:51392\n"
    "global_load_dwordx4 v[232:235], v133, s[42:43] offset:3072 sc1\n"
    "s_add_u32 s42, s42, 18432\n"
    "s_addc_u32 s43, s43, 0\n"
    "v_exp_f32 v54, v54\n"
    "v_exp_f32 v55, v55\n"
    "v_exp_f32 v56, v56\n"
    "v_exp_f32 v57, v57\n"
    "v_smfmac_f32_32x32x32_bf16 v[8:23], v[124:127], v[48:55], v128\n"
    "global_load_dwordx4 v[212:215], v133, s[42:43] offset:0 sc1\n"
    "v_exp_f32 v58, v58\n"
    "v_exp_f32 v59, v59\n"
    "v_exp_f32 v60, v60\n"
    "v_exp_f32 v61, v61\n"
    "v_mfma_f32_32x32x16_bf16 v[72:87], v[114:117], v[104:107], v[72:87]\n"
    "global_load_dwordx4 v[220:223], v133, s[42:43] offset:1024 sc1\n"
    "v_exp_f32 v62, v62\n"
    "v_exp_f32 v63, v63\n"
    "v_exp_f32 v64, v64\n"
    "v_exp_f32 v65, v65\n"
    "v_smfmac_f32_32x32x32_bf16 v[24:39], v[120:123], v[56:63], v128\n"
    "ds_read_b128 v[120:123], v131 offset:49056\n"
    "global_load_dwordx4 v[228:231], v133, s[42:43] offset:2048 sc1\n"
    "v_exp_f32 v66, v66\n"
    "v_exp_f32 v67, v67\n"
    "v_exp_f32 v68, v68\n"
    "v_mfma_f32_32x32x16_bf16 v[88:103], v[114:117], v[108:111], v[88:103]\n"
    "ds_read_b64_tr_b16 v[114:115], v129 offset:51392\n"
    "ds_read_b64_tr_b16 v[116:117], v129 offset:51456\n"
    "v_exp_f32 v69, v69\n"
    "v_exp_f32 v70, v70\n"
    "v_exp_f32 v71, v71\n"
    "v_exp_f32 v72, v72\n"
    "v_exp_f32 v73, v73\n"
    "v_smfmac_f32_32x32x32_bf16 v[24:39], v[124:127], v[64:71], v128\n"
    "s_waitcnt lgkmcnt(2)\n"
    "ds_read_b128 v[124:127], v132 offset:49056\n"
    "s_setprio 1\n"
    "v_exp_f32 v74, v74\n"
    "v_exp_f32 v75, v75\n"
    "v_exp_f32 v76, v76\n"
    "v_exp_f32 v77, v77\n"
    "v_mfma_f32_32x32x4_2b_bf16 v[40:71], v[118:119], v[112:113], 0\n"
    "v_exp_f32 v78, v78\n"
    "v_exp_f32 v79, v79\n"
    "v_exp_f32 v80, v80\n"
    "v_exp_f32 v81, v81\n"
    "v_exp_f32 v82, v82\n"
    "v_exp_f32 v83, v83\n"
    "v_exp_f32 v84, v84\n"
    "v_exp_f32 v85, v85\n"
    "v_smfmac_f32_32x32x32_bf16 v[8:23], v[120:123], v[72:79], v128\n"
    "s_waitcnt lgkmcnt(0)\n"
    "ds_read_b64_tr_b16 v[118:119], v130 offset:53728\n"
    "global_load_dwordx4 v[236:239], v133, s[42:43] offset:3072 sc1\n"
    "v_exp_f32 v86, v86\n"
    "v_exp_f32 v87, v87\n"
    "v_exp_f32 v88, v88\n"
    "v_exp_f32 v89, v89\n"
    "v_smfmac_f32_32x32x32_bf16 v[8:23], v[124:127], v[80:87], v128\n"
    "s_sub_u32 s42, s42, 10240\n"
    "s_subb_u32 s43, s43, 0\n"
    "global_load_dwordx4 v[240:243], v133, s[42:43] sc1\n"
    "v_exp_f32 v90, v90\n"
    "v_exp_f32 v91, v91\n"
    "v_exp_f32 v92, v92\n"
    "v_exp_f32 v93, v93\n"
    "v_mfma_f32_32x32x16_bf16 v[40:55], v[114:117], v[104:107], v[40:55]\n"
    "v_exp_f32 v94, v94\n"
    "v_exp_f32 v95, v95\n"
    "v_exp_f32 v96, v96\n"
    "v_exp_f32 v97, v97\n"
    "v_smfmac_f32_32x32x32_bf16 v[24:39], v[120:123], v[88:95], v128\n"
    "ds_read_b128 v[120:123], v131 offset:51392\n"
    "v_exp_f32 v98, v98\n"
    "v_exp_f32 v99, v99\n"
    "v_exp_f32 v100, v100\n"
    "v_mfma_f32_32x32x16_bf16 v[56:71], v[114:117], v[108:111], v[56:71]\n"
    "ds_read_b64_tr_b16 v[114:115], v129 offset:53728\n"
    "ds_read_b64_tr_b16 v[116:117], v129 offset:53792\n"
    "v_exp_f32 v101, v101\n"
    "v_exp_f32 v102, v102\n"
    "v_exp_f32 v103, v103\n"
    "v_exp_f32 v40, v40\n"
    "v_exp_f32 v41, v41\n"
    "v_smfmac_f32_32x32x32_bf16 v[24:39], v[124:127], v[96:103], v128\n"
    "s_waitcnt lgkmcnt(2)\n"
    "ds_read_b128 v[124:127], v132 offset:51392\n"
    "v_exp_f32 v42, v42\n"
    "v_exp_f32 v43, v43\n"
    "v_exp_f32 v44, v44\n"
    "v_exp_f32 v45, v45\n"
    "v_mfma_f32_32x32x4_2b_bf16 v[72:103], v[118:119], v[112:113], 0\n"
    "v_exp_f32 v46, v46\n"
    "v_exp_f32 v47, v47\n"
    "v_exp_f32 v48, v48\n"
    "v_exp_f32 v49, v49\n"
    "v_exp_f32 v50, v50\n"
    "v_exp_f32 v51, v51\n"
    "v_exp_f32 v52, v52\n"
    "v_exp_f32 v53, v53\n"
    "v_smfmac_f32_32x32x32_bf16 v[8:23], v[120:123], v[40:47], v128\n"
    "s_waitcnt lgkmcnt(0)\n"
    "v_exp_f32 v54, v54\n"
    "v_exp_f32 v55, v55\n"
    "v_exp_f32 v56, v56\n"
    "v_exp_f32 v57, v57\n"
    "v_smfmac_f32_32x32x32_bf16 v[8:23], v[124:127], v[48:55], v128\n"
    "v_exp_f32 v58, v58\n"
    "v_exp_f32 v59, v59\n"
    "v_exp_f32 v60, v60\n"
    "v_exp_f32 v61, v61\n"
    "v_mfma_f32_32x32x16_bf16 v[72:87], v[114:117], v[104:107], v[72:87]\n"
    "v_exp_f32 v62, v62\n"
    "v_exp_f32 v63, v63\n"
    "v_exp_f32 v64, v64\n"
    "v_exp_f32 v65, v65\n"
    "v_smfmac_f32_32x32x32_bf16 v[24:39], v[120:123], v[56:63], v128\n"
    "ds_read_b128 v[120:123], v131 offset:53728\n"
    "v_exp_f32 v66, v66\n"
    "v_exp_f32 v67, v67\n"
    "v_exp_f32 v68, v68\n"
    "v_mfma_f32_32x32x16_bf16 v[88:103], v[114:117], v[108:111], v[88:103]\n"
    "v_exp_f32 v69, v69\n"
    "v_exp_f32 v70, v70\n"
    "v_exp_f32 v71, v71\n"
    "v_exp_f32 v72, v72\n"
    "v_exp_f32 v73, v73\n"
    "v_smfmac_f32_32x32x32_bf16 v[24:39], v[124:127], v[64:71], v128\n"
    "s_waitcnt lgkmcnt(0)\n"
    "ds_read_b128 v[124:127], v132 offset:53728\n"
    "s_setprio 0\n"
    "v_exp_f32 v74, v74\n"
    "v_exp_f32 v75, v75\n"
    "v_exp_f32 v76, v76\n"
    "v_exp_f32 v77, v77\n"
    "v_exp_f32 v78, v78\n"
    "v_exp_f32 v79, v79\n"
    "v_exp_f32 v80, v80\n"
    "v_exp_f32 v81, v81\n"
    "v_exp_f32 v82, v82\n"
    "v_exp_f32 v83, v83\n"
    "v_exp_f32 v84, v84\n"
    "v_exp_f32 v85, v85\n"
    "v_smfmac_f32_32x32x32_bf16 v[8:23], v[120:123], v[72:79], v128\n"
    "s_waitcnt lgkmcnt(0)\n"
    "v_exp_f32 v86, v86\n"
    "v_exp_f32 v87, v87\n"
    "v_exp_f32 v88, v88\n"
    "v_exp_f32 v89, v89\n"
    "v_smfmac_f32_32x32x32_bf16 v[8:23], v[124:127], v[80:87], v128\n"
    "v_exp_f32 v90, v90\n"
    "v_exp_f32 v91, v91\n"
    "v_exp_f32 v92, v92\n"
    "v_exp_f32 v93, v93\n"
    "v_exp_f32 v94, v94\n"
    "v_exp_f32 v95, v95\n"
    "v_exp_f32 v96, v96\n"
    "v_exp_f32 v97, v97\n"
    "v_smfmac_f32_32x32x32_bf16 v[24:39], v[120:123], v[88:95], v128\n"
    "v_exp_f32 v98, v98\n"
    "v_exp_f32 v99, v99\n"
    "v_exp_f32 v100, v100\n"
    "v_exp_f32 v101, v101\n"
    "v_exp_f32 v102, v102\n"
    "v_exp_f32 v103, v103\n"
    "v_cvt_pk_bf16_f32 v40, v8, v9\n"
    "v_cvt_pk_bf16_f32 v41, v10, v11\n"
    "v_cvt_pk_bf16_f32 v42, v12, v13\n"
    "v_cvt_pk_bf16_f32 v43, v14, v15\n"
    "v_cvt_pk_bf16_f32 v44, v16, v17\n"
    "v_mov_b32 v45, v18\n"
    "v_smfmac_f32_32x32x32_bf16 v[24:39], v[124:127], v[96:103], v128\n"
    "ds_write_b64 v246, v[40:41] offset:0\n"
    "ds_write_b64 v246, v[42:43] offset:512\n"
    "ds_write_b64 v246, v[44:45] offset:1024\n"
    "v_xor_b32 v246, 0x2000, v246\n"
    "s_waitcnt vmcnt(9) lgkmcnt(0)\n"
    "s_barrier\n"
    "ds_read_b64 v[44:45], v246 offset:1024\n"
    "ds_read_b64 v[40:41], v246 offset:0\n"
    "ds_read_b64 v[42:43], v246 offset:512\n"
    "s_nop 1\n"
    "s_waitcnt lgkmcnt(2)\n"
    "v_add_f32 v34, v34, v45\n"
    "v_mov_b32 v46, v34\n"
    "v_lshlrev_b32 v56, 16, v44\n"
    "v_and_b32 v57, 0xffff0000, v44\n"
    "v_permlane32_swap_b32 v34, v46\n"
    "v_pk_add_f32 v[32:33], v[32:33], v[56:57]\n"
    "s_nop 0\n"
    "v_rcp_f32 v46, v34\n"
    "s_waitcnt lgkmcnt(0)\n"
    "v_lshlrev_b32 v48, 16, v40\n"
    "v_and_b32 v49, 0xffff0000, v40\n"
    "v_pk_add_f32 v[24:25], v[24:25], v[48:49]\n"
    "v_lshlrev_b32 v50, 16, v41\n"
    "v_and_b32 v51, 0xffff0000, v41\n"
    "v_pk_add_f32 v[26:27], v[26:27], v[50:51]\n"
    "v_lshlrev_b32 v52, 16, v42\n"
    "v_and_b32 v53, 0xffff0000, v42\n"
    "v_pk_add_f32 v[28:29], v[28:29], v[52:53]\n"
    "v_lshlrev_b32 v54, 16, v43\n"
    "v_and_b32 v55, 0xffff0000, v43\n"
    "v_pk_add_f32 v[30:31], v[30:31], v[54:55]\n"
    "v_pk_mul_f32 v[24:25], v[46:47], v[24:25] op_sel:[0,0] op_sel_hi:[0,1]\n"
    "v_cvt_pk_bf16_f32 v40, v24, v25\n"
    "v_pk_mul_f32 v[26:27], v[46:47], v[26:27] op_sel:[0,0] op_sel_hi:[0,1]\n"
    "v_cvt_pk_bf16_f32 v41, v26, v27\n"
    "v_pk_mul_f32 v[28:29], v[46:47], v[28:29] op_sel:[0,0] op_sel_hi:[0,1]\n"
    "v_cvt_pk_bf16_f32 v42, v28, v29\n"
    "v_pk_mul_f32 v[30:31], v[46:47], v[30:31] op_sel:[0,0] op_sel_hi:[0,1]\n"
    "v_cvt_pk_bf16_f32 v43, v30, v31\n"
    "global_store_dwordx4 v247, v[40:43], %[ws] sc1\n"
    "v_pk_mul_f32 v[32:33], v[46:47], v[32:33] op_sel:[0,0] op_sel_hi:[0,1]\n"
    "v_cvt_pk_bf16_f32 v48, v32, v33\n"
    "s_nop 1\n"
    "v_mov_b32_dpp v49, v48 quad_perm:[1,1,1,1] row_mask:0xf bank_mask:0xf\n"
    "v_mov_b32_dpp v50, v48 quad_perm:[2,2,2,2] row_mask:0xf bank_mask:0xf\n"
    "v_mov_b32_dpp v51, v48 quad_perm:[3,3,3,3] row_mask:0xf bank_mask:0xf\n"
    "s_mov_b64 exec, 0x11111111\n"
    "global_store_dwordx4 v248, v[48:51], %[ws] sc1\n"
    "s_mov_b64 exec, -1\n"
    "v_add_f32 v4, |v24|, |v25|\n"
    "v_add_f32 v4, v4, |v26|\n"
    "v_add_f32 v4, v4, |v27|\n"
    "v_add_f32 v4, v4, |v28|\n"
    "v_add_f32 v4, v4, |v29|\n"
    "v_add_f32 v4, v4, |v30|\n"
    "v_add_f32 v4, v4, |v31|\n"
    "v_add_f32 v4, v4, |v32|\n"
    "v_add_f32 v4, v4, |v33|\n"
    "v_mov_b32 v5, v34\n"
        : "+{v[136:167]}"(uu), "={v[168:199]}"(cf1), "={v[200:207]}"(cf2), "+{v[208:239]}"(sp), "+{v[240:243]}"(wf1), "+{v[128:135]}"(mi),
          "+{v[244:251]}"(ta), "+{v[0:7]}"(tb), "+{s[40:41]}"(g), "+{s[42:43]}"(wfg), "+{s[44:45]}"(hp), [ldst] "+s"(ldst),
          [cnt] "=&s"(cnt)
        : "{s[46:47]}"(tag), [ne] "s"(ne), [jo] "s"(300 - 36 * ne), [wq] "s"(wq), [w] "s"(w), [fh] "s"(hasU ? WS_FH + myf : 0),
          [cg] "s"(kh ? P.cim : P.cre), [lq] "s"(lq), [lc] "s"(kh * L_G1 + wq * 4096), [ws] "s"(ws)
        : "memory", "scc", "vcc", "v8", "v9", "v10", "v11", "v12", "v13", "v14", "v15", "v16", "v17", "v18", "v19", "v20", "v21", "v22", "v23", "v24", "v25", "v26", "v27", "v28", "v29", "v30", "v31", "v32", "v33", "v34", "v35", "v36", "v37", "v38", "v39", "v40", "v41", "v42", "v43", "v44", "v45", "v46", "v47", "v48", "v49", "v50", "v51", "v52", "v53", "v54", "v55", "v56", "v57", "v58", "v59", "v60", "v61", "v62", "v63", "v64", "v65", "v66", "v67", "v68", "v69", "v70", "v71", "v72", "v73", "v74", "v75", "v76", "v77", "v78", "v79", "v80", "v81", "v82", "v83", "v84", "v85", "v86", "v87", "v88", "v89", "v90", "v91", "v92", "v93", "v94", "v95", "v96", "v97", "v98", "v99", "v100", "v101", "v102", "v103", "v104", "v105", "v106", "v107", "v108", "v109", "v110", "v111", "v112", "v113", "v114", "v115", "v116", "v117", "v118", "v119", "v120", "v121", "v122", "v123", "v124", "v125", "v126", "v127", "v252", "v253", "v254", "s48", "s49", "s52", "s53", "s54", "s55");
  }
  lane = mi[6] >> 2;
  if (__builtin_expect(__builtin_amdgcn_ballot_w64((asf(tb[5]) >= 0x1p-40f) & (asf(tb[5]) <= 0x1p80f) & (asf(tb[4]) <= 0x1p126f)) != ~0ull, 0)) {
    h = lane >> 5;
    const int t = t32 * 32 + (lane & 31);
    float qv[18], ov[18], mx = -__builtin_inff(), ls = 0.f;
#pragma unroll
    for (int i = 0; i < 18; ++i) {
      qv[i] = bflo(__hip_atomic_load((const AS1 unsigned short*)(ws + head * 288 + (t >> 3) * QS + i * 16 + (t & 7) * 2), __ATOMIC_RELAXED, __HIP_MEMORY_SCOPE_AGENT));
      ov[i] = 0.f;
    }
#pragma nounroll
    for (int pass = 0; pass < 2; ++pass) {
#pragma nounroll
      for (int kp = 0; kp < 512; ++kp) {
        const char* kb = kvg + (h * 128 + (kp >> 2)) * QS + (kp & 3) * 4;
        float s0 = 0.f, s1 = 0.f;
#pragma unroll
        for (int i = 0; i < 18; ++i) {
          const unsigned x = __hip_atomic_load((const AS1 unsigned*)(kb + i * 16), __ATOMIC_RELAXED, __HIP_MEMORY_SCOPE_AGENT);
          s0 = fmaf(qv[i], bflo(x), s0); s1 = fmaf(qv[i], bfhi(x), s1);
        }
        if (pass == 0) {
          mx = fmaxf(mx, fmaxf(s0, s1));
        } else {
          s0 = __builtin_amdgcn_exp2f(s0 - mx); s1 = __builtin_amdgcn_exp2f(s1 - mx);
          ls += s0 + s1;
#pragma unroll
          for (int i = 0; i < 18; ++i) {
            const unsigned x = __hip_atomic_load((const AS1 unsigned*)(kb + 9216 + i * 16), __ATOMIC_RELAXED, __HIP_MEMORY_SCOPE_AGENT);
            ov[i] = fmaf(s0, bflo(x), fmaf(s1, bfhi(x), ov[i]));
          }
        }
      }
      mx = fmaxf(mx, __shfl_xor(mx, 32));
    }
    const float inv = 1.0f / (ls + __shfl_xor(ls, 32));
#pragma unroll
    for (int i = 0; i < 18; ++i) ov[i] = (ov[i] + __shfl_xor(ov[i], 32)) * inv;
    u32x4 m;
#pragma unroll
    for (int i = 0; i < 4; ++i) {
      const int a = 8 * (i >> 1) + 2 * (i & 1);
      m[i] = h ? pkbf(ov[a + 4], ov[a + 5]) : pkbf(ov[a], ov[a + 1]);
    }
    st16(ws, ta[3], m);
    const unsigned ex = pkbf(ov[16], ov[17]);
    const u32x4 ex4 = {ex, (unsigned)__builtin_amdgcn_mov_dpp(ex, 0x55, 0xf, 0xf, true), (unsigned)__builtin_amdgcn_mov_dpp(ex, 0xaa, 0xf, 0xf, true),
                       (unsigned)__builtin_amdgcn_mov_dpp(ex, 0xff, 0xf, 0xf, true)};
    if (!h && !(lane & 3)) st16(ws, ta[4], ex4);
    wait_vm<0>();
  }
  const unsigned fo_off = WS_FO + myf;
  if (!hasT) {
    wait_vm<0>();
    if (lane == 0) st8(ws, fo_off, tag);
    return;
  }
  {
    const char *hp = ws + hq, *ogb = ws + og, *xsk = P.xb + (t0 + 4 * w) * 1152 + ct3 * 128;
    const char *outa = (const char*)P.out + (t0 + 4 * w) * 4608 + ct3 * 256, *outb = outa + 2304;
    unsigned cnt;
    asm volatile(
    "s_waitcnt lgkmcnt(0)\n"
    "ds_read_b64_tr_b16 v[48:49], v249 offset:0\n"
    "ds_read_b64_tr_b16 v[50:51], v249 offset:64\n"
    "ds_read_b64_tr_b16 v[52:53], v249 offset:512\n"
    "ds_read_b64_tr_b16 v[54:55], v249 offset:576\n"
    "ds_read_b64_tr_b16 v[56:57], v249 offset:1024\n"
    "ds_read_b64_tr_b16 v[58:59], v249 offset:1088\n"
    "ds_read_b64_tr_b16 v[60:61], v249 offset:1536\n"
    "ds_read_b64_tr_b16 v[62:63], v249 offset:1600\n"
    "s_add_u32 s44, s44, 110592\n"
    "s_addc_u32 s45, s45, 0\n"
    "s_add_u32 m0, %[lq], 55680\n"
    "s_nop 0\n"
    "global_load_lds_dwordx4 v133, s[44:45] offset:0 sc1\n"
    "s_waitcnt lgkmcnt(6)\n"
    "v_mfma_f32_16x16x32_bf16 v[8:11], v[136:139], v[48:51], 0\n"
    "ds_read_b64_tr_b16 v[64:65], v249 offset:2048\n"
    "ds_read_b64_tr_b16 v[66:67], v249 offset:2112\n"
    "v_mfma_f32_16x16x32_bf16 v[12:15], v[140:143], v[48:51], 0\n"
    "global_load_lds_dwordx4 v133, s[44:45] offset:1024 sc1\n"
    "s_waitcnt lgkmcnt(6)\n"
    "v_mfma_f32_16x16x32_bf16 v[8:11], v[144:147], v[52:55], v[8:11]\n"
    "ds_read_b64_tr_b16 v[48:49], v249 offset:2560\n"
    "ds_read_b64_tr_b16 v[50:51], v249 offset:2624\n"
    "v_mfma_f32_16x16x32_bf16 v[12:15], v[148:151], v[52:55], v[12:15]\n"
    "global_load_lds_dwordx4 v133, s[44:45] offset:2048 sc1\n"
    "s_waitcnt lgkmcnt(6)\n"
    "v_mfma_f32_16x16x32_bf16 v[8:11], v[152:155], v[56:59], v[8:11]\n"
    "ds_read_b64_tr_b16 v[52:53], v249 offset:3072\n"
    "ds_read_b64_tr_b16 v[54:55], v249 offset:3136\n"
    "v_mfma_f32_16x16x32_bf16 v[12:15], v[156:159], v[56:59], v[12:15]\n"
    "s_waitcnt lgkmcnt(6)\n"
    "v_mfma_f32_16x16x32_bf16 v[8:11], v[160:163], v[60:63], v[8:11]\n"
    "ds_read_b64_tr_b16 v[56:57], v249 offset:3584\n"
    "ds_read_b64_tr_b16 v[58:59], v249 offset:3648\n"
    "v_mfma_f32_16x16x32_bf16 v[12:15], v[164:167], v[60:63], v[12:15]\n"
    "global_load_lds_dwordx4 v133, s[44:45] offset:3072 sc1\n"
    "s_add_u32 s44, s44, 4096\n"
    "s_addc_u32 s45, s45, 0\n"
    "s_mul_i32 s49, %[w], 3\n"
    "s_min_u32 s49, s49, 15\n"
    "s_lshl_b32 s49, s49, 10\n"
    "s_waitcnt lgkmcnt(6)\n"
    "v_mfma_f32_16x16x32_bf16 v[8:11], v[168:171], v[64:67], v[8:11]\n"
    "ds_read_b64_tr_b16 v[60:61], v249 offset:4096\n"
    "ds_read_b64_tr_b16 v[62:63], v249 offset:4160\n"
    "v_mfma_f32_16x16x32_bf16 v[12:15], v[172:175], v[64:67], v[12:15]\n"
    "s_add_u32 s40, s42, s49\n"
    "s_addc_u32 s41, s43, 0\n"
    "s_add_u32 s40, s40, 73728\n"
    "s_addc_u32 s41, s41, 0\n"
    "s_and_b32 s62, %[w], 3\n"
    "s_waitcnt lgkmcnt(6)\n"
    "v_mfma_f32_16x16x32_bf16 v[8:11], v[176:179], v[48:51], v[8:11]\n"
    "ds_read_b64_tr_b16 v[64:65], v249 offset:18560\n"
    "ds_read_b64_tr_b16 v[66:67], v249 offset:18624\n"
    "v_mfma_f32_16x16x32_bf16 v[12:15], v[180:183], v[48:51], v[12:15]\n"
    "s_add_u32 m0, %[lq], 59776\n"
    "s_nop 0\n"
    "global_load_lds_dwordx4 v133, s[44:45] offset:0 sc1\n"
    "s_add_u32 s44, s44, 32768\n"
    "s_addc_u32 s45, s45, 0\n"
    "s_mul_i32 s60, s62, 5\n"
    "s_min_u32 s60, s60, 13\n"
    "s_lshl_b32 s60, s60, 10\n"
    "s_waitcnt lgkmcnt(6)\n"
    "v_mfma_f32_16x16x32_bf16 v[8:11], v[184:187], v[52:55], v[8:11]\n"
    "ds_read_b64_tr_b16 v[48:49], v249 offset:19072\n"
    "ds_read_b64_tr_b16 v[50:51], v249 offset:19136\n"
    "v_mfma_f32_16x16x32_bf16 v[12:15], v[188:191], v[52:55], v[12:15]\n"
    "s_mul_i32 s54, %[kh], 18432\n"
    "s_add_u32 s54, s54, s60\n"
    "s_add_u32 s42, s42, s54\n"
    "s_addc_u32 s43, s43, 0\n"
    "v_mov_b32 v252, s46\n"
    "v_mov_b32 v253, s47\n"
    "v_mov_b32 v254, %[fo]\n"
    "s_waitcnt vmcnt(5)\n"
    "s_mov_b64 exec, 1\n"
    "global_store_dwordx2 v254, v[252:253], %[ws] sc1\n"
    "s_mov_b64 exec, -1\n"
    "s_waitcnt lgkmcnt(6)\n"
    "v_mfma_f32_16x16x32_bf16 v[8:11], v[192:195], v[56:59], v[8:11]\n"
    "ds_read_b64_tr_b16 v[52:53], v249 offset:19584\n"
    "ds_read_b64_tr_b16 v[54:55], v249 offset:19648\n"
    "v_mfma_f32_16x16x32_bf16 v[12:15], v[196:199], v[56:59], v[12:15]\n"
    "global_load_dwordx4 v[112:115], v133, %[wx] sc1\n"
    "s_mul_i32 s63, %[kh], 74752\n"
    "s_add_u32 s60, s60, s63\n"
    "s_waitcnt lgkmcnt(6)\n"
    "v_mfma_f32_16x16x32_bf16 v[8:11], v[200:203], v[60:63], v[8:11]\n"
    "ds_read_b64_tr_b16 v[56:57], v249 offset:20096\n"
    "ds_read_b64_tr_b16 v[58:59], v249 offset:20160\n"
    "v_mfma_f32_16x16x32_bf16 v[12:15], v[204:207], v[60:63], v[12:15]\n"
    "s_waitcnt lgkmcnt(6)\n"
    "v_mfma_f32_16x16x32_bf16 v[16:19], v[136:139], v[64:67], 0\n"
    "ds_read_b64_tr_b16 v[60:61], v249 offset:20608\n"
    "ds_read_b64_tr_b16 v[62:63], v249 offset:20672\n"
    "v_mfma_f32_16x16x32_bf16 v[20:23], v[140:143], v[64:67], 0\n"
    "s_barrier\n"
    "v_bfe_u32 v84, v133, 4, 4\n"
    "v_lshrrev_b32 v85, 8, v133\n"
    "s_and_b32 s54, %[w], 1\n"
    "s_waitcnt lgkmcnt(6)\n"
    "v_mfma_f32_16x16x32_bf16 v[16:19], v[144:147], v[48:51], v[16:19]\n"
    "ds_read_b64_tr_b16 v[64:65], v249 offset:21120\n"
    "ds_read_b64_tr_b16 v[66:67], v249 offset:21184\n"
    "v_mfma_f32_16x16x32_bf16 v[20:23], v[148:151], v[48:51], v[20:23]\n"
    "s_add_u32 m0, %[lq], 0\n"
    "s_nop 0\n"
    "global_load_lds_dwordx4 v133, s[44:45] offset:0 sc1\n"
    "s_lshl_b32 s54, s54, 3\n"
    "v_and_b32 v86, 7, v84\n"
    "v_bitop3_b32 v86, v86, v85, s54 bitop3:0x96\n"
    "s_waitcnt lgkmcnt(6)\n"
    "v_mfma_f32_16x16x32_bf16 v[16:19], v[152:155], v[52:55], v[16:19]\n"
    "ds_read_b64_tr_b16 v[48:49], v249 offset:21632\n"
    "ds_read_b64_tr_b16 v[50:51], v249 offset:21696\n"
    "v_mfma_f32_16x16x32_bf16 v[20:23], v[156:159], v[52:55], v[20:23]\n"
    "s_lshr_b32 s54, %[w], 1\n"
    "s_lshl_b32 s54, s54, 12\n"
    "v_lshlrev_b32 v86, 4, v86\n"
    "s_waitcnt lgkmcnt(6)\n"
    "v_mfma_f32_16x16x32_bf16 v[16:19], v[160:163], v[56:59], v[16:19]\n"
    "ds_read_b64_tr_b16 v[52:53], v249 offset:22144\n"
    "ds_read_b64_tr_b16 v[54:55], v249 offset:22208\n"
    "v_mfma_f32_16x16x32_bf16 v[20:23], v[164:167], v[56:59], v[20:23]\n"
    "global_load_lds_dwordx4 v133, s[44:45] offset:1024 sc1\n"
    "v_lshl_or_b32 v86, v84, 8, v86\n"
    "v_add_u32 v100, s54, v86\n"
    "v_xor_b32 v101, 64, v100\n"
    "s_waitcnt lgkmcnt(6)\n"
    "v_mfma_f32_16x16x32_bf16 v[16:19], v[168:171], v[60:63], v[16:19]\n"
    "ds_read_b64_tr_b16 v[56:57], v249 offset:22656\n"
    "ds_read_b64_tr_b16 v[58:59], v249 offset:22720\n"
    "v_mfma_f32_16x16x32_bf16 v[20:23], v[172:175], v[60:63], v[20:23]\n"
    "v_add_u32 v102, 74752, v100\n"
    "v_add_u32 v103, 74752, v101\n"
    "s_lshl_b32 s54, s62, 2\n"
    "s_waitcnt lgkmcnt(6)\n"
    "v_mfma_f32_16x16x32_bf16 v[16:19], v[176:179], v[64:67], v[16:19]\n"
    "ds_read_b64_tr_b16 v[60:61], v249 offset:37120\n"
    "ds_read_b64_tr_b16 v[62:63], v249 offset:37184\n"
    "v_mfma_f32_16x16x32_bf16 v[20:23], v[180:183], v[64:67], v[20:23]\n"
    "global_load_lds_dwordx4 v133, s[44:45] offset:2048 sc1\n"
    "v_add_u32 v86, s54, v85\n"
    "v_bitop3_b32 v87, v84, v86, 7 bitop3:0x78\n"
    "v_lshlrev_b32 v87, 4, v87\n"
    "s_waitcnt lgkmcnt(6)\n"
    "v_mfma_f32_16x16x32_bf16 v[16:19], v[184:187], v[48:51], v[16:19]\n"
    "ds_read_b64_tr_b16 v[64:65], v249 offset:37632\n"
    "ds_read_b64_tr_b16 v[66:67], v249 offset:37696\n"
    "v_mfma_f32_16x16x32_bf16 v[20:23], v[188:191], v[48:51], v[20:23]\n"
    "s_add_u32 s54, s63, 18688\n"
    "v_lshl_or_b32 v87, v86, 8, v87\n"
    "v_add_u32 v104, s54, v87\n"
    "s_waitcnt lgkmcnt(6)\n"
    "v_mfma_f32_16x16x32_bf16 v[16:19], v[192:195], v[52:55], v[16:19]\n"
    "ds_read_b64_tr_b16 v[48:49], v249 offset:38144\n"
    "ds_read_b64_tr_b16 v[50:51], v249 offset:38208\n"
    "v_mfma_f32_16x16x32_bf16 v[20:23], v[196:199], v[52:55], v[20:23]\n"
    "global_load_lds_dwordx4 v133, s[44:45] offset:3072 sc1\n"
    "s_add_u32 s44, s44, 4096\n"
    "s_addc_u32 s45, s45, 0\n"
    "s_lshl_b32 s54, %[w], 1\n"
    "v_and_or_b32 v86, v85, 1, s54\n"
    "v_bitop3_b32 v87, v84, v86, 7 bitop3:0x78\n"
    "s_waitcnt lgkmcnt(6)\n"
    "v_mfma_f32_16x16x32_bf16 v[16:19], v[200:203], v[56:59], v[16:19]\n"
    "ds_read_b64_tr_b16 v[52:53], v249 offset:38656\n"
    "ds_read_b64_tr_b16 v[54:55], v249 offset:38720\n"
    "v_mfma_f32_16x16x32_bf16 v[20:23], v[204:207], v[56:59], v[20:23]\n"
    "v_lshlrev_b32 v87, 4, v87\n"
    "v_lshl_or_b32 v87, v86, 8, v87\n"
    "v_add_u32 v105, 146944, v87\n"
    "s_waitcnt lgkmcnt(6)\n"
    "v_mfma_f32_16x16x32_bf16 v[24:27], v[136:139], v[60:63], 0\n"
    "ds_read_b64_tr_b16 v[56:57], v249 offset:39168\n"
    "ds_read_b64_tr_b16 v[58:59], v249 offset:39232\n"
    "v_mfma_f32_16x16x32_bf16 v[28:31], v[140:143], v[60:63], 0\n"
    "s_add_u32 m0, %[lq], 4096\n"
    "s_nop 0\n"
    "global_load_lds_dwordx4 v133, s[44:45] offset:0 sc1\n"
    "v_mul_u32_u24 v106, 0x1200, v85\n"
    "v_lshl_add_u32 v106, v84, 4, v106\n"
    "s_lshr_b32 s54, s62, 1\n"
    "s_waitcnt lgkmcnt(6)\n"
    "v_mfma_f32_16x16x32_bf16 v[24:27], v[144:147], v[64:67], v[24:27]\n"
    "ds_read_b64_tr_b16 v[60:61], v249 offset:39680\n"
    "ds_read_b64_tr_b16 v[62:63], v249 offset:39744\n"
    "v_mfma_f32_16x16x32_bf16 v[28:31], v[148:151], v[64:67], v[28:31]\n"
    "s_lshl_b32 s62, s54, 1\n"
    "s_add_u32 s62, s62, %[kh]\n"
    "s_lshl_b32 s62, s62, 12\n"
    "s_waitcnt lgkmcnt(6)\n"
    "v_mfma_f32_16x16x32_bf16 v[24:27], v[152:155], v[48:51], v[24:27]\n"
    "ds_read_b64_tr_b16 v[64:65], v249 offset:40192\n"
    "ds_read_b64_tr_b16 v[66:67], v249 offset:40256\n"
    "v_mfma_f32_16x16x32_bf16 v[28:31], v[156:159], v[48:51], v[28:31]\n"
    "global_load_dwordx2 v[76:77], v0, %[dsk]\n"
    "global_load_dwordx2 v[78:79], v251, s[50:51]\n"
    "s_add_u32 s50, s50, 36864\n"
    "s_addc_u32 s51, s51, 0\n"
    "v_add_u32 v107, s62, v133\n"
    "v_add_u32 v108, 74752, v107\n"
    "s_lshl_b32 s54, s54, 10\n"
    "s_waitcnt lgkmcnt(6)\n"
    "v_mfma_f32_16x16x32_bf16 v[24:27], v[160:163], v[52:55], v[24:27]\n"
    "ds_read_b64_tr_b16 v[48:49], v249 offset:40704\n"
    "ds_read_b64_tr_b16 v[50:51], v249 offset:40768\n"
    "v_mfma_f32_16x16x32_bf16 v[28:31], v[164:167], v[52:55], v[28:31]\n"
    "global_load_dwordx2 v[80:81], v251, s[50:51]\n"
    "s_mul_i32 s55, %[w], 2304\n"
    "s_sub_u32 s55, 36864, s55\n"
    "s_add_u32 s50, s50, s55\n"
    "s_addc_u32 s51, s51, 0\n"
    "s_add_u32 s54, s54, 16384\n"
    "v_add_u32 v109, s54, v134\n"
    "v_add_u32 v110, 74752, v109\n"
    "s_waitcnt lgkmcnt(6)\n"
    "v_mfma_f32_16x16x32_bf16 v[24:27], v[168:171], v[56:59], v[24:27]\n"
    "ds_read_b64_tr_b16 v[52:53], v249 offset:41216\n"
    "ds_read_b64_tr_b16 v[54:55], v249 offset:41280\n"
    "v_mfma_f32_16x16x32_bf16 v[28:31], v[172:175], v[56:59], v[28:31]\n"
    "s_mov_b32 exec_hi, 0\n"
    "global_load_dwordx2 v[82:83], v251, s[50:51]\n"
    "s_mov_b32 exec_hi, -1\n"
    "s_mul_i32 s61, %[w], 9216\n"
    "s_sub_u32 s61, 147456, s61\n"
    "s_waitcnt lgkmcnt(6)\n"
    "v_mfma_f32_16x16x32_bf16 v[24:27], v[176:179], v[60:63], v[24:27]\n"
    "s_waitcnt vmcnt(11)\n"
    "s_barrier\n"
    "ds_read_b64_tr_b16 v[56:57], v249 offset:55680\n"
    "ds_read_b64_tr_b16 v[58:59], v249 offset:55744\n"
    "v_mfma_f32_16x16x32_bf16 v[28:31], v[180:183], v[60:63], v[28:31]\n"
    "s_waitcnt lgkmcnt(6)\n"
    "v_mfma_f32_16x16x32_bf16 v[24:27], v[184:187], v[64:67], v[24:27]\n"
    "ds_read_b64_tr_b16 v[60:61], v249 offset:56192\n"
    "ds_read_b64_tr_b16 v[62:63], v249 offset:56256\n"
    "v_mfma_f32_16x16x32_bf16 v[28:31], v[188:191], v[64:67], v[28:31]\n"
    "ds_write_b128 v100, v[8:11] offset:18688\n"
    "s_waitcnt lgkmcnt(7)\n"
    "v_mfma_f32_16x16x32_bf16 v[24:27], v[192:195], v[48:51], v[24:27]\n"
    "ds_read_b64_tr_b16 v[64:65], v249 offset:56704\n"
    "ds_read_b64_tr_b16 v[66:67], v249 offset:56768\n"
    "v_mfma_f32_16x16x32_bf16 v[28:31], v[196:199], v[48:51], v[28:31]\n"
    "ds_write_b128 v101, v[12:15] offset:18688\n"
    "s_waitcnt lgkmcnt(8)\n"
    "v_mfma_f32_16x16x32_bf16 v[24:27], v[200:203], v[52:55], v[24:27]\n"
    "ds_read_b64_tr_b16 v[48:49], v249 offset:57216\n"
    "ds_read_b64_tr_b16 v[50:51], v249 offset:57280\n"
    "v_mfma_f32_16x16x32_bf16 v[28:31], v[204:207], v[52:55], v[28:31]\n"
    "ds_write_b128 v102, v[16:19] offset:18688\n"
    "s_cmp_gt_u32 %[w], 1\n"
    "s_cbranch_scc1 7f\n"
    "global_load_dwordx2 v[252:253], v250, %[ws] sc1\n"
    "7:\n"
    "s_waitcnt lgkmcnt(9)\n"
    "v_mfma_f32_16x16x32_bf16 v[32:35], v[136:139], v[56:59], 0\n"
    "ds_read_b64_tr_b16 v[52:53], v249 offset:57728\n"
    "ds_read_b64_tr_b16 v[54:55], v249 offset:57792\n"
    "v_mfma_f32_16x16x32_bf16 v[36:39], v[140:143], v[56:59], 0\n"
    "ds_write_b128 v103, v[20:23] offset:18688\n"
    "s_waitcnt lgkmcnt(10)\n"
    "v_mfma_f32_16x16x32_bf16 v[32:35], v[144:147], v[60:63], v[32:35]\n"
    "ds_read_b64_tr_b16 v[56:57], v249 offset:58240\n"
    "ds_read_b64_tr_b16 v[58:59], v249 offset:58304\n"
    "v_mfma_f32_16x16x32_bf16 v[36:39], v[148:151], v[60:63], v[36:39]\n"
    "s_waitcnt lgkmcnt(9)\n"
    "v_mfma_f32_16x16x32_bf16 v[32:35], v[152:155], v[64:67], v[32:35]\n"
    "ds_read_b64_tr_b16 v[60:61], v249 offset:58752\n"
    "ds_read_b64_tr_b16 v[62:63], v249 offset:58816\n"
    "v_mfma_f32_16x16x32_bf16 v[36:39], v[156:159], v[64:67], v[36:39]\n"
    "s_waitcnt lgkmcnt(8)\n"
    "v_mfma_f32_16x16x32_bf16 v[32:35], v[160:163], v[48:51], v[32:35]\n"
    "ds_read_b64_tr_b16 v[64:65], v249 offset:59264\n"
    "ds_read_b64_tr_b16 v[66:67], v249 offset:59328\n"
    "v_mfma_f32_16x16x32_bf16 v[36:39], v[164:167], v[48:51], v[36:39]\n"
    "s_waitcnt lgkmcnt(7)\n"
    "v_mfma_f32_16x16x32_bf16 v[32:35], v[168:171], v[52:55], v[32:35]\n"
    "ds_read_b64_tr_b16 v[48:49], v249 offset:59776\n"
    "ds_read_b64_tr_b16 v[50:51], v249 offset:59840\n"
    "v_mfma_f32_16x16x32_bf16 v[36:39], v[172:175], v[52:55], v[36:39]\n"
    "s_waitcnt lgkmcnt(6)\n"
    "v_mfma_f32_16x16x32_bf16 v[32:35], v[176:179], v[56:59], v[32:35]\n"
    "s_waitcnt vmcnt(0)\n"
    "v_cmp_ne_u64 vcc, s[46:47], v[252:253]\n"
    "s_cbranch_vccz 5f\n"
    "s_mov_b32 %[cnt], 0x40000\n"
    "6:\n"
    "s_sleep 2\n"
    "global_load_dwordx2 v[252:253], v250, %[ws] sc1\n"
    "s_waitcnt vmcnt(0)\n"
    "v_cmp_ne_u64 vcc, s[46:47], v[252:253]\n"
    "s_sub_u32 %[cnt], %[cnt], 1\n"
    "s_cbranch_scc1 5f\n"
    "s_cbranch_vccnz 6b\n"
    "5:\n"
    "s_barrier\n"
    "ds_read_b128 v[8:11], v104 offset:0\n"
    "ds_read_b128 v[12:15], v104 offset:4096\n"
    "ds_read_b128 v[16:19], v104 offset:8192\n"
    "ds_read_b128 v[20:23], v104 offset:12288\n"
    "ds_read_b64_tr_b16 v[52:53], v249 offset:0\n"
    "ds_read_b64_tr_b16 v[54:55], v249 offset:64\n"
    "v_mfma_f32_16x16x32_bf16 v[36:39], v[180:183], v[56:59], v[36:39]\n"
    "s_add_u32 m0, s60, 37120\n"
    "s_nop 0\n"
    "global_load_lds_dwordx4 v133, s[42:43] offset:0 sc1\n"
    "s_waitcnt lgkmcnt(10)\n"
    "v_mfma_f32_16x16x32_bf16 v[32:35], v[184:187], v[60:63], v[32:35]\n"
    "ds_read_b64_tr_b16 v[56:57], v249 offset:512\n"
    "ds_read_b64_tr_b16 v[58:59], v249 offset:576\n"
    "v_mfma_f32_16x16x32_bf16 v[36:39], v[188:191], v[60:63], v[36:39]\n"
    "v_lshlrev_b32 v84, 16, v76\n"
    "v_and_b32 v85, 0xffff0000, v76\n"
    "v_lshlrev_b32 v86, 16, v77\n"
    "v_and_b32 v87, 0xffff0000, v77\n"
    "v_lshlrev_b32 v88, 16, v78\n"
    "global_load_lds_dwordx4 v133, s[42:43] offset:1024 sc1\n"
    "s_waitcnt lgkmcnt(10)\n"
    "v_mfma_f32_16x16x32_bf16 v[32:35], v[192:195], v[64:67], v[32:35]\n"
    "ds_read_b64_tr_b16 v[60:61], v249 offset:1024\n"
    "ds_read_b64_tr_b16 v[62:63], v249 offset:1088\n"
    "v_mfma_f32_16x16x32_bf16 v[36:39], v[196:199], v[64:67], v[36:39]\n"
    "v_and_b32 v89, 0xffff0000, v78\n"
    "v_lshlrev_b32 v90, 16, v79\n"
    "v_and_b32 v91, 0xffff0000, v79\n"
    "v_mul_f32 v88, v88, v84\n"
    "v_mul_f32 v89, v89, v85\n"
    "s_waitcnt lgkmcnt(10)\n"
    "v_mfma_f32_16x16x32_bf16 v[32:35], v[200:203], v[48:51], v[32:35]\n"
    "ds_read_b64_tr_b16 v[64:65], v249 offset:1536\n"
    "ds_read_b64_tr_b16 v[66:67], v249 offset:1600\n"
    "v_mfma_f32_16x16x32_bf16 v[36:39], v[204:207], v[48:51], v[36:39]\n"
    "v_mul_f32 v90, v90, v86\n"
    "v_mul_f32 v91, v91, v87\n"
    "v_cvt_pk_bf16_f32 v78, v88, v89\n"
    "v_cvt_pk_bf16_f32 v79, v90, v91\n"
    "v_lshlrev_b32 v88, 16, v78\n"
    "global_load_lds_dwordx4 v133, s[42:43] offset:2048 sc1\n"
    "s_waitcnt lgkmcnt(6)\n"
    "v_mfma_f32_16x16x32_bf16 v[40:43], v[136:139], v[52:55], 0\n"
    "ds_read_b64_tr_b16 v[48:49], v249 offset:2048\n"
    "ds_read_b64_tr_b16 v[50:51], v249 offset:2112\n"
    "v_mfma_f32_16x16x32_bf16 v[44:47], v[140:143], v[52:55], 0\n"
    "v_and_b32 v89, 0xffff0000, v78\n"
    "v_lshlrev_b32 v90, 16, v79\n"
    "v_and_b32 v91, 0xffff0000, v79\n"
    "s_barrier\n"
    "global_load_lds_dwordx4 v133, s[42:43] offset:3072 sc1\n"
    "s_add_u32 s42, s42, 4096\n"
    "s_addc_u32 s43, s43, 0\n"
    "v_add_f32 v8, v8, v12\n"
    "v_add_f32 v9, v9, v13\n"
    "v_add_f32 v10, v10, v14\n"
    "v_add_f32 v11, v11, v15\n"
    "s_waitcnt lgkmcnt(6)\n"
    "v_mfma_f32_16x16x32_bf16 v[40:43], v[144:147], v[56:59], v[40:43]\n"
    "ds_read_b64_tr_b16 v[52:53], v249 offset:2560\n"
    "ds_read_b64_tr_b16 v[54:55], v249 offset:2624\n"
    "v_mfma_f32_16x16x32_bf16 v[44:47], v[148:151], v[56:59], v[44:47]\n"
    "ds_write_b128 v100, v[24:27] offset:55808\n"
    "v_add_f32 v16, v16, v20\n"
    "v_add_f32 v17, v17, v21\n"
    "v_add_f32 v18, v18, v22\n"
    "v_add_f32 v19, v19, v23\n"
    "s_waitcnt lgkmcnt(7)\n"
    "v_mfma_f32_16x16x32_bf16 v[40:43], v[152:155], v[60:63], v[40:43]\n"
    "ds_read_b64_tr_b16 v[56:57], v249 offset:3072\n"
    "ds_read_b64_tr_b16 v[58:59], v249 offset:3136\n"
    "v_mfma_f32_16x16x32_bf16 v[44:47], v[156:159], v[60:63], v[44:47]\n"
    "s_add_u32 m0, s60, 41216\n"
    "s_nop 0\n"
    "global_load_lds_dwordx4 v133, s[42:43] offset:0 sc1\n"
    "s_add_u32 s42, s42, 32768\n"
    "s_addc_u32 s43, s43, 0\n"
    "ds_write_b128 v101, v[28:31] offset:55808\n"
    "v_add_f32 v88, v88, v8\n"
    "v_add_f32 v89, v89, v9\n"
    "v_add_f32 v90, v90, v10\n"
    "v_add_f32 v91, v91, v11\n"
    "s_waitcnt lgkmcnt(8)\n"
    "v_mfma_f32_16x16x32_bf16 v[40:43], v[160:163], v[64:67], v[40:43]\n"
    "ds_read_b64_tr_b16 v[60:61], v249 offset:3584\n"
    "ds_read_b64_tr_b16 v[62:63], v249 offset:3648\n"
    "v_mfma_f32_16x16x32_bf16 v[44:47], v[164:167], v[64:67], v[44:47]\n"
    "s_add_u32 m0, s60, 18688\n"
    "s_nop 0\n"
    "global_load_lds_dwordx4 v133, s[42:43] offset:0 sc1\n"
    "ds_write_b128 v102, v[32:35] offset:55808\n"
    "v_add_f32 v88, v88, v16\n"
    "v_add_f32 v89, v89, v17\n"
    "v_add_f32 v90, v90, v18\n"
    "v_add_f32 v91, v91, v19\n"
    "s_waitcnt lgkmcnt(9)\n"
    "v_mfma_f32_16x16x32_bf16 v[40:43], v[168:171], v[48:51], v[40:43]\n"
    "ds_read_b64_tr_b16 v[64:65], v249 offset:4096\n"
    "ds_read_b64_tr_b16 v[66:67], v249 offset:4160\n"
    "v_mfma_f32_16x16x32_bf16 v[44:47], v[172:175], v[48:51], v[44:47]\n"
    "v_lshlrev_b32 v92, 16, v80\n"
    "v_and_b32 v93, 0xffff0000, v80\n"
    "v_lshlrev_b32 v94, 16, v81\n"
    "v_and_b32 v95, 0xffff0000, v81\n"
    "v_mul_f32 v92, v92, v84\n"
    "v_mul_f32 v93, v93, v85\n"
    "v_mul_f32 v94, v94, v86\n"
    "global_load_lds_dwordx4 v133, s[42:43] offset:1024 sc1\n"
    "ds_write_b128 v103, v[36:39] offset:55808\n"
    "s_waitcnt lgkmcnt(10)\n"
    "v_mfma_f32_16x16x32_bf16 v[40:43], v[176:179], v[52:55], v[40:43]\n"
    "v_mfma_f32_16x16x32_bf16 v[44:47], v[180:183], v[52:55], v[44:47]\n"
    "v_mul_f32 v95, v95, v87\n"
    "v_cvt_pk_bf16_f32 v80, v92, v93\n"
    "v_cvt_pk_bf16_f32 v81, v94, v95\n"
    "v_lshlrev_b32 v92, 16, v80\n"
    "v_and_b32 v93, 0xffff0000, v80\n"
    "v_lshlrev_b32 v94, 16, v81\n"
    "v_and_b32 v95, 0xffff0000, v81\n"
    "global_load_lds_dwordx4 v133, s[42:43] offset:2048 sc1\n"
    "s_waitcnt lgkmcnt(7)\n"
    "v_mfma_f32_16x16x32_bf16 v[40:43], v[184:187], v[56:59], v[40:43]\n"
    "v_mfma_f32_16x16x32_bf16 v[44:47], v[188:191], v[56:59], v[44:47]\n"
    "v_lshlrev_b32 v96, 16, v82\n"
    "v_and_b32 v97, 0xffff0000, v82\n"
    "v_lshlrev_b32 v98, 16, v83\n"
    "v_and_b32 v99, 0xffff0000, v83\n"
    "v_mul_f32 v96, v96, v84\n"
    "v_mul_f32 v97, v97, v85\n"
    "v_mul_f32 v98, v98, v86\n"
    "global_load_lds_dwordx4 v133, s[42:43] offset:3072 sc1\n"
    "s_add_u32 s42, s42, 4096\n"
    "s_addc_u32 s43, s43, 0\n"
    "s_waitcnt lgkmcnt(4)\n"
    "v_mfma_f32_16x16x32_bf16 v[40:43], v[192:195], v[60:63], v[40:43]\n"
    "v_mfma_f32_16x16x32_bf16 v[44:47], v[196:199], v[60:63], v[44:47]\n"
    "v_mul_f32 v99, v99, v87\n"
    "v_cvt_pk_bf16_f32 v82, v96, v97\n"
    "v_cvt_pk_bf16_f32 v83, v98, v99\n"
    "v_lshlrev_b32 v96, 16, v82\n"
    "v_and_b32 v97, 0xffff0000, v82\n"
    "v_lshlrev_b32 v98, 16, v83\n"
    "v_and_b32 v99, 0xffff0000, v83\n"
    "s_add_u32 m0, s60, 22784\n"
    "s_nop 0\n"
    "global_load_lds_dwordx4 v133, s[42:43] offset:0 sc1\n"
    "global_store_dwordx4 v106, v[88:91], s[56:57] sc1\n"
    "s_add_u32 s56, s56, 147456\n"
    "s_addc_u32 s57, s57, 0\n"
    "s_waitcnt lgkmcnt(1)\n"
    "v_mfma_f32_16x16x32_bf16 v[40:43], v[200:203], v[64:67], v[40:43]\n"
    "s_waitcnt vmcnt(6)\n"
    "s_waitcnt lgkmcnt(0)\n"
    "s_barrier\n"
    "v_mfma_f32_16x16x32_bf16 v[44:47], v[204:207], v[64:67], v[44:47]\n"
    "s_cmp_eq_u32 %[kh], 0\n"
    "s_cbranch_scc0 30f\n"
    "ds_read_b128 v[48:51], v107 offset:37120\n"
    "ds_read_b128 v[52:55], v107 offset:38144\n"
    "ds_read_b128 v[56:59], v107 offset:39168\n"
    "s_waitcnt lgkmcnt(2)\n"
    "v_mfma_f32_16x16x32_bf16 v[8:11], v[208:211], v[48:51], 0\n"
    "ds_read_b128 v[60:63], v107 offset:40192\n"
    "v_mfma_f32_16x16x32_bf16 v[12:15], v[212:215], v[48:51], 0\n"
    "ds_read_b128 v[136:139], v104 offset:37120\n"
    "ds_read_b128 v[140:143], v104 offset:41216\n"
    "ds_read_b128 v[144:147], v104 offset:45312\n"
    "ds_read_b128 v[148:151], v104 offset:49408\n"
    "s_add_u32 m0, s49, 0\n"
    "s_nop 0\n"
    "global_load_lds_dwordx4 v133, s[40:41] offset:0 sc1\n"
    "s_waitcnt lgkmcnt(6)\n"
    "v_mfma_f32_16x16x32_bf16 v[8:11], v[216:219], v[52:55], v[8:11]\n"
    "ds_read2st64_b32 v[48:49], v109 offset0:145 offset1:146\n"
    "ds_read2st64_b32 v[50:51], v109 offset0:147 offset1:148\n"
    "v_mfma_f32_16x16x32_bf16 v[12:15], v[220:223], v[52:55], v[12:15]\n"
    "v_add_u32 v102, 16384, v102\n"
    "v_add_u32 v103, 16384, v103\n"
    "ds_write_b128 v102, v[40:43] offset:55808\n"
    "ds_write_b128 v103, v[44:47] offset:55808\n"
    "v_subrev_u32 v102, 16384, v102\n"
    "v_subrev_u32 v103, 16384, v103\n"
    "global_load_lds_dwordx4 v133, s[40:41] offset:1024 sc1\n"
    "s_waitcnt lgkmcnt(9)\n"
    "v_mfma_f32_16x16x32_bf16 v[8:11], v[224:227], v[56:59], v[8:11]\n"
    "ds_read_b128 v[52:55], v108 offset:37120\n"
    "v_mfma_f32_16x16x32_bf16 v[12:15], v[228:231], v[56:59], v[12:15]\n"
    "global_load_lds_dwordx4 v133, s[40:41] offset:2048 sc1\n"
    "v_add_u32 v76, 18432, v104\n"
    "s_waitcnt lgkmcnt(9)\n"
    "v_mfma_f32_16x16x32_bf16 v[8:11], v[232:235], v[60:63], v[8:11]\n"
    "ds_read_b128 v[56:59], v108 offset:38144\n"
    "v_mfma_f32_16x16x32_bf16 v[12:15], v[236:239], v[60:63], v[12:15]\n"
    "s_waitcnt lgkmcnt(4)\n"
    "v_mfma_f32_16x16x32_bf16 v[8:11], v[240:243], v[48:51], v[8:11]\n"
    "ds_read_b128 v[60:63], v108 offset:39168\n"
    "v_mfma_f32_16x16x32_bf16 v[12:15], v[112:115], v[48:51], v[12:15]\n"
    "v_add_f32 v136, v136, v140\n"
    "v_add_f32 v137, v137, v141\n"
    "v_add_f32 v138, v138, v142\n"
    "v_add_f32 v139, v139, v143\n"
    "v_add_f32 v144, v144, v148\n"
    "v_add_f32 v145, v145, v149\n"
    "s_waitcnt lgkmcnt(2)\n"
    "v_mfma_f32_16x16x32_bf16 v[16:19], v[208:211], v[52:55], 0\n"
    "ds_read_b128 v[48:51], v108 offset:40192\n"
    "v_mfma_f32_16x16x32_bf16 v[20:23], v[212:215], v[52:55], 0\n"
    "v_add_f32 v146, v146, v150\n"
    "v_add_f32 v147, v147, v151\n"
    "v_add_f32 v92, v92, v136\n"
    "v_add_f32 v93, v93, v137\n"
    "v_add_f32 v94, v94, v138\n"
    "v_add_f32 v95, v95, v139\n"
    "s_waitcnt lgkmcnt(2)\n"
    "v_mfma_f32_16x16x32_bf16 v[16:19], v[216:219], v[56:59], v[16:19]\n"
    "ds_read2st64_b32 v[52:53], v110 offset0:145 offset1:146\n"
    "ds_read2st64_b32 v[54:55], v110 offset0:147 offset1:148\n"
    "v_mfma_f32_16x16x32_bf16 v[20:23], v[220:223], v[56:59], v[20:23]\n"
    "v_add_f32 v92, v92, v144\n"
    "v_add_f32 v93, v93, v145\n"
    "v_add_f32 v94, v94, v146\n"
    "v_add_f32 v95, v95, v147\n"
    "global_store_dwordx4 v106, v[92:95], s[56:57] sc1\n"
    "s_add_u32 s56, s56, s61\n"
    "s_addc_u32 s57, s57, 0\n"
    "s_waitcnt lgkmcnt(3)\n"
    "v_mfma_f32_16x16x32_bf16 v[16:19], v[224:227], v[60:63], v[16:19]\n"
    "s_waitcnt vmcnt(5)\n"
    "s_barrier\n"
    "ds_read_b128 v[152:155], v105 offset:0\n"
    "ds_read_b128 v[156:159], v105 offset:4096\n"
    "ds_read_b128 v[160:163], v105 offset:8192\n"
    "ds_read_b128 v[164:167], v105 offset:12288\n"
    "ds_read_b128 v[56:59], v107 offset:18688\n"
    "v_mfma_f32_16x16x32_bf16 v[20:23], v[228:231], v[60:63], v[20:23]\n"
    "s_waitcnt lgkmcnt(7)\n"
    "v_mfma_f32_16x16x32_bf16 v[16:19], v[232:235], v[48:51], v[16:19]\n"
    "ds_read_b128 v[60:63], v107 offset:19712\n"
    "v_mfma_f32_16x16x32_bf16 v[20:23], v[236:239], v[48:51], v[20:23]\n"
    "ds_write_b128 v100, v[8:11] offset:37120\n"
    "ds_write_b128 v101, v[12:15] offset:37120\n"
    "s_waitcnt lgkmcnt(8)\n"
    "v_mfma_f32_16x16x32_bf16 v[16:19], v[240:243], v[52:55], v[16:19]\n"
    "ds_read_b128 v[48:51], v107 offset:20736\n"
    "v_mfma_f32_16x16x32_bf16 v[20:23], v[112:115], v[52:55], v[20:23]\n"
    "s_waitcnt lgkmcnt(4)\n"
    "v_mfma_f32_16x16x32_bf16 v[24:27], v[208:211], v[56:59], 0\n"
    "ds_read_b128 v[52:55], v107 offset:21760\n"
    "v_mfma_f32_16x16x32_bf16 v[28:31], v[212:215], v[56:59], 0\n"
    "v_add_f32 v152, v152, v156\n"
    "v_add_f32 v153, v153, v157\n"
    "v_add_f32 v154, v154, v158\n"
    "v_add_f32 v155, v155, v159\n"
    "v_add_f32 v160, v160, v164\n"
    "v_add_f32 v161, v161, v165\n"
    "s_waitcnt lgkmcnt(4)\n"
    "v_mfma_f32_16x16x32_bf16 v[24:27], v[216:219], v[60:63], v[24:27]\n"
    "ds_read2st64_b32 v[56:57], v109 offset0:73 offset1:74\n"
    "ds_read2st64_b32 v[58:59], v109 offset0:75 offset1:76\n"
    "v_mfma_f32_16x16x32_bf16 v[28:31], v[220:223], v[60:63], v[28:31]\n"
    "v_add_f32 v162, v162, v166\n"
    "v_add_f32 v163, v163, v167\n"
    "v_add_f32 v96, v96, v152\n"
    "v_add_f32 v97, v97, v153\n"
    "v_add_f32 v98, v98, v154\n"
    "v_add_f32 v99, v99, v155\n"
    "ds_write_b128 v102, v[16:19] offset:0\n"
    "ds_write_b128 v103, v[20:23] offset:0\n"
    "s_waitcnt lgkmcnt(5)\n"
    "v_mfma_f32_16x16x32_bf16 v[24:27], v[224:227], v[48:51], v[24:27]\n"
    "ds_read_b128 v[60:63], v108 offset:18688\n"
    "v_mfma_f32_16x16x32_bf16 v[28:31], v[228:231], v[48:51], v[28:31]\n"
    "v_add_f32 v96, v96, v160\n"
    "v_add_f32 v97, v97, v161\n"
    "v_add_f32 v98, v98, v162\n"
    "v_add_f32 v99, v99, v163\n"
    "s_mov_b32 exec_hi, 0\n"
    "global_store_dwordx4 v106, v[96:99], s[56:57] sc1\n"
    "s_mov_b32 exec_hi, -1\n"
    "s_waitcnt lgkmcnt(5)\n"
    "v_mfma_f32_16x16x32_bf16 v[24:27], v[232:235], v[52:55], v[24:27]\n"
    "ds_read_b128 v[48:51], v108 offset:19712\n"
    "v_mfma_f32_16x16x32_bf16 v[28:31], v[236:239], v[52:55], v[28:31]\n"
    "s_waitcnt lgkmcnt(4)\n"
    "v_mfma_f32_16x16x32_bf16 v[24:27], v[240:243], v[56:59], v[24:27]\n"
    "ds_read_b128 v[52:55], v108 offset:20736\n"
    "v_mfma_f32_16x16x32_bf16 v[28:31], v[112:115], v[56:59], v[28:31]\n"
    "s_waitcnt lgkmcnt(2)\n"
    "v_mfma_f32_16x16x32_bf16 v[32:35], v[208:211], v[60:63], 0\n"
    "ds_read_b128 v[56:59], v108 offset:21760\n"
    "v_mfma_f32_16x16x32_bf16 v[36:39], v[212:215], v[60:63], 0\n"
    "s_waitcnt lgkmcnt(2)\n"
    "v_mfma_f32_16x16x32_bf16 v[32:35], v[216:219], v[48:51], v[32:35]\n"
    "ds_read2st64_b32 v[60:61], v110 offset0:73 offset1:74\n"
    "ds_read2st64_b32 v[62:63], v110 offset0:75 offset1:76\n"
    "v_mfma_f32_16x16x32_bf16 v[36:39], v[220:223], v[48:51], v[36:39]\n"
    "s_waitcnt lgkmcnt(3)\n"
    "v_mfma_f32_16x16x32_bf16 v[32:35], v[224:227], v[52:55], v[32:35]\n"
    "s_waitcnt vmcnt(2)\n"
    "s_barrier\n"
    "ds_read_b128 v[136:139], v76 offset:0\n"
    "ds_read_b128 v[140:143], v76 offset:4096\n"
    "ds_read_b128 v[144:147], v76 offset:8192\n"
    "ds_read_b128 v[148:151], v76 offset:12288\n"
    "ds_read_b128 v[48:51], v107 offset:0\n"
    "v_mfma_f32_16x16x32_bf16 v[36:39], v[228:231], v[52:55], v[36:39]\n"
    "s_waitcnt lgkmcnt(7)\n"
    "v_mfma_f32_16x16x32_bf16 v[32:35], v[232:235], v[56:59], v[32:35]\n"
    "ds_read_b128 v[52:55], v107 offset:1024\n"
    "v_mfma_f32_16x16x32_bf16 v[36:39], v[236:239], v[56:59], v[36:39]\n"
    "ds_write_b128 v100, v[24:27] offset:55808\n"
    "ds_write_b128 v101, v[28:31] offset:55808\n"
    "s_waitcnt lgkmcnt(8)\n"
    "v_mfma_f32_16x16x32_bf16 v[32:35], v[240:243], v[60:63], v[32:35]\n"
    "ds_read_b128 v[56:59], v107 offset:2048\n"
    "v_mfma_f32_16x16x32_bf16 v[36:39], v[112:115], v[60:63], v[36:39]\n"
    "s_waitcnt lgkmcnt(4)\n"
    "v_mfma_f32_16x16x32_bf16 v[40:43], v[208:211], v[48:51], 0\n"
    "ds_read_b128 v[60:63], v107 offset:3072\n"
    "v_mfma_f32_16x16x32_bf16 v[44:47], v[212:215], v[48:51], 0\n"
    "v_add_f32 v136, v136, v140\n"
    "v_add_f32 v137, v137, v141\n"
    "v_add_f32 v138, v138, v142\n"
    "v_add_f32 v139, v139, v143\n"
    "v_add_f32 v144, v144, v148\n"
    "v_add_f32 v145, v145, v149\n"
    "v_add_f32 v146, v146, v150\n"
    "s_waitcnt lgkmcnt(4)\n"
    "v_mfma_f32_16x16x32_bf16 v[40:43], v[216:219], v[52:55], v[40:43]\n"
    "ds_read2st64_b32 v[48:49], v109 offset0:0 offset1:1\n"
    "ds_read2st64_b32 v[50:51], v109 offset0:2 offset1:3\n"
    "v_mfma_f32_16x16x32_bf16 v[44:47], v[220:223], v[52:55], v[44:47]\n"
    "v_add_f32 v147, v147, v151\n"
    "v_add_f32 v136, v136, v144\n"
    "v_add_f32 v137, v137, v145\n"
    "v_add_f32 v138, v138, v146\n"
    "v_add_f32 v139, v139, v147\n"
    "v_cvt_pk_bf16_f32 v140, v136, v137\n"
    "v_cvt_pk_bf16_f32 v141, v138, v139\n"
    "s_waitcnt lgkmcnt(3)\n"
    "v_mfma_f32_16x16x32_bf16 v[40:43], v[224:227], v[56:59], v[40:43]\n"
    "v_mfma_f32_16x16x32_bf16 v[44:47], v[228:231], v[56:59], v[44:47]\n"
    "ds_write_b128 v102, v[32:35] offset:55808\n"
    "ds_write_b128 v103, v[36:39] offset:55808\n"
    "v_add_u32 v102, 16384, v102\n"
    "v_add_u32 v103, 16384, v103\n"
    "v_lshlrev_b32 v136, 16, v140\n"
    "v_and_b32 v137, 0xffff0000, v140\n"
    "v_lshlrev_b32 v138, 16, v141\n"
    "v_and_b32 v139, 0xffff0000, v141\n"
    "global_store_dwordx4 v106, v[136:139], s[58:59] sc1\n"
    "s_add_u32 s58, s58, 147456\n"
    "s_addc_u32 s59, s59, 0\n"
    "s_waitcnt lgkmcnt(4)\n"
    "v_mfma_f32_16x16x32_bf16 v[40:43], v[232:235], v[60:63], v[40:43]\n"
    "v_mfma_f32_16x16x32_bf16 v[44:47], v[236:239], v[60:63], v[44:47]\n"
    "s_waitcnt lgkmcnt(2)\n"
    "v_mfma_f32_16x16x32_bf16 v[40:43], v[240:243], v[48:51], v[40:43]\n"
    "v_mfma_f32_16x16x32_bf16 v[44:47], v[112:115], v[48:51], v[44:47]\n"
    "s_nop 6\n"
    "s_branch 31f\n"
    "30:\n"
    "ds_read_b128 v[48:51], v107 offset:37120\n"
    "ds_read_b128 v[52:55], v107 offset:38144\n"
    "ds_read_b128 v[56:59], v107 offset:39168\n"
    "s_waitcnt lgkmcnt(2)\n"
    "v_mfma_f32_16x16x32_bf16 v[8:11], v[208:211], v[48:51], 0\n"
    "ds_read_b128 v[60:63], v107 offset:40192\n"
    "v_mfma_f32_16x16x32_bf16 v[12:15], v[212:215], v[48:51], 0\n"
    "ds_read_b128 v[136:139], v104 offset:37120\n"
    "ds_read_b128 v[140:143], v104 offset:41216\n"
    "ds_read_b128 v[144:147], v104 offset:45312\n"
    "ds_read_b128 v[148:151], v104 offset:49408\n"
    "s_add_u32 m0, s49, 0\n"
    "s_nop 0\n"
    "global_load_lds_dwordx4 v133, s[40:41] offset:0 sc1\n"
    "s_waitcnt lgkmcnt(6)\n"
    "v_mfma_f32_16x16x32_bf16 v[8:11], v[216:219], v[52:55], v[8:11]\n"
    "ds_read_b128 v[48:51], v108 offset:37120\n"
    "v_mfma_f32_16x16x32_bf16 v[12:15], v[220:223], v[52:55], v[12:15]\n"
    "v_add_u32 v102, 16384, v102\n"
    "v_add_u32 v103, 16384, v103\n"
    "ds_write_b128 v102, v[40:43] offset:55808\n"
    "ds_write_b128 v103, v[44:47] offset:55808\n"
    "v_subrev_u32 v102, 16384, v102\n"
    "v_subrev_u32 v103, 16384, v103\n"
    "global_load_lds_dwordx4 v133, s[40:41] offset:1024 sc1\n"
    "s_waitcnt lgkmcnt(8)\n"
    "v_mfma_f32_16x16x32_bf16 v[8:11], v[224:227], v[56:59], v[8:11]\n"
    "ds_read_b128 v[52:55], v108 offset:38144\n"
    "v_mfma_f32_16x16x32_bf16 v[12:15], v[228:231], v[56:59], v[12:15]\n"
    "global_load_lds_dwordx4 v133, s[40:41] offset:2048 sc1\n"
    "v_add_u32 v76, -18688, v104\n"
    "s_waitcnt lgkmcnt(8)\n"
    "v_mfma_f32_16x16x32_bf16 v[8:11], v[232:235], v[60:63], v[8:11]\n"
    "ds_read_b128 v[56:59], v108 offset:39168\n"
    "v_mfma_f32_16x16x32_bf16 v[12:15], v[236:239], v[60:63], v[12:15]\n"
    "s_waitcnt lgkmcnt(4)\n"
    "v_mfma_f32_16x16x32_bf16 v[16:19], v[208:211], v[48:51], 0\n"
    "ds_read_b128 v[60:63], v108 offset:40192\n"
    "v_mfma_f32_16x16x32_bf16 v[20:23], v[212:215], v[48:51], 0\n"
    "v_add_f32 v136, v136, v140\n"
    "v_add_f32 v137, v137, v141\n"
    "v_add_f32 v138, v138, v142\n"
    "v_add_f32 v139, v139, v143\n"
    "v_add_f32 v144, v144, v148\n"
    "v_add_f32 v145, v145, v149\n"
    "s_waitcnt lgkmcnt(2)\n"
    "v_mfma_f32_16x16x32_bf16 v[16:19], v[216:219], v[52:55], v[16:19]\n"
    "s_waitcnt vmcnt(4)\n"
    "s_barrier\n"
    "ds_read_b128 v[152:155], v105 offset:0\n"
    "ds_read_b128 v[156:159], v105 offset:4096\n"
    "ds_read_b128 v[160:163], v105 offset:8192\n"
    "ds_read_b128 v[164:167], v105 offset:12288\n"
    "ds_read_b128 v[48:51], v107 offset:18688\n"
    "v_mfma_f32_16x16x32_bf16 v[20:23], v[220:223], v[52:55], v[20:23]\n"
    "v_add_f32 v146, v146, v150\n"
    "v_add_f32 v147, v147, v151\n"
    "v_add_f32 v92, v92, v136\n"
    "v_add_f32 v93, v93, v137\n"
    "v_add_f32 v94, v94, v138\n"
    "v_add_f32 v95, v95, v139\n"
    "s_waitcnt lgkmcnt(6)\n"
    "v_mfma_f32_16x16x32_bf16 v[16:19], v[224:227], v[56:59], v[16:19]\n"
    "ds_read_b128 v[52:55], v107 offset:19712\n"
    "v_mfma_f32_16x16x32_bf16 v[20:23], v[228:231], v[56:59], v[20:23]\n"
    "v_add_f32 v92, v92, v144\n"
    "v_add_f32 v93, v93, v145\n"
    "v_add_f32 v94, v94, v146\n"
    "v_add_f32 v95, v95, v147\n"
    "global_store_dwordx4 v106, v[92:95], s[56:57] sc1\n"
    "s_add_u32 s56, s56, s61\n"
    "s_addc_u32 s57, s57, 0\n"
    "ds_write_b128 v100, v[8:11] offset:37120\n"
    "ds_write_b128 v101, v[12:15] offset:37120\n"
    "s_waitcnt lgkmcnt(8)\n"
    "v_mfma_f32_16x16x32_bf16 v[16:19], v[232:235], v[60:63], v[16:19]\n"
    "ds_read_b128 v[56:59], v107 offset:20736\n"
    "v_mfma_f32_16x16x32_bf16 v[20:23], v[236:239], v[60:63], v[20:23]\n"
    "s_waitcnt lgkmcnt(4)\n"
    "v_mfma_f32_16x16x32_bf16 v[24:27], v[208:211], v[48:51], 0\n"
    "ds_read_b128 v[60:63], v107 offset:21760\n"
    "v_mfma_f32_16x16x32_bf16 v[28:31], v[212:215], v[48:51], 0\n"
    "v_add_f32 v152, v152, v156\n"
    "v_add_f32 v153, v153, v157\n"
    "v_add_f32 v154, v154, v158\n"
    "v_add_f32 v155, v155, v159\n"
    "v_add_f32 v160, v160, v164\n"
    "v_add_f32 v161, v161, v165\n"
    "s_waitcnt lgkmcnt(4)\n"
    "v_mfma_f32_16x16x32_bf16 v[24:27], v[216:219], v[52:55], v[24:27]\n"
    "ds_read_b128 v[48:51], v108 offset:18688\n"
    "v_mfma_f32_16x16x32_bf16 v[28:31], v[220:223], v[52:55], v[28:31]\n"
    "v_add_f32 v162, v162, v166\n"
    "v_add_f32 v163, v163, v167\n"
    "v_add_f32 v96, v96, v152\n"
    "v_add_f32 v97, v97, v153\n"
    "v_add_f32 v98, v98, v154\n"
    "v_add_f32 v99, v99, v155\n"
    "ds_write_b128 v102, v[16:19] offset:0\n"
    "ds_write_b128 v103, v[20:23] offset:0\n"
    "s_waitcnt lgkmcnt(4)\n"
    "v_mfma_f32_16x16x32_bf16 v[24:27], v[224:227], v[56:59], v[24:27]\n"
    "ds_read_b128 v[52:55], v108 offset:19712\n"
    "v_mfma_f32_16x16x32_bf16 v[28:31], v[228:231], v[56:59], v[28:31]\n"
    "v_add_f32 v96, v96, v160\n"
    "v_add_f32 v97, v97, v161\n"
    "v_add_f32 v98, v98, v162\n"
    "v_add_f32 v99, v99, v163\n"
    "s_mov_b32 exec_hi, 0\n"
    "global_store_dwordx4 v106, v[96:99], s[56:57] sc1\n"
    "s_mov_b32 exec_hi, -1\n"
    "s_waitcnt lgkmcnt(4)\n"
    "v_mfma_f32_16x16x32_bf16 v[24:27], v[232:235], v[60:63], v[24:27]\n"
    "ds_read_b128 v[56:59], v108 offset:20736\n"
    "v_mfma_f32_16x16x32_bf16 v[28:31], v[236:239], v[60:63], v[28:31]\n"
    "s_waitcnt lgkmcnt(4)\n"
    "v_mfma_f32_16x16x32_bf16 v[32:35], v[208:211], v[48:51], 0\n"
    "ds_read_b128 v[60:63], v108 offset:21760\n"
    "v_mfma_f32_16x16x32_bf16 v[36:39], v[212:215], v[48:51], 0\n"
    "s_waitcnt lgkmcnt(2)\n"
    "v_mfma_f32_16x16x32_bf16 v[32:35], v[216:219], v[52:55], v[32:35]\n"
    "s_waitcnt vmcnt(2)\n"
    "s_barrier\n"
    "ds_read_b128 v[136:139], v76 offset:0\n"
    "ds_read_b128 v[140:143], v76 offset:4096\n"
    "ds_read_b128 v[144:147], v76 offset:8192\n"
    "ds_read_b128 v[148:151], v76 offset:12288\n"
    "ds_read_b128 v[48:51], v107 offset:0\n"
    "v_mfma_f32_16x16x32_bf16 v[36:39], v[220:223], v[52:55], v[36:39]\n"
    "s_waitcnt lgkmcnt(6)\n"
    "v_mfma_f32_16x16x32_bf16 v[32:35], v[224:227], v[56:59], v[32:35]\n"
    "ds_read_b128 v[52:55], v107 offset:1024\n"
    "v_mfma_f32_16x16x32_bf16 v[36:39], v[228:231], v[56:59], v[36:39]\n"
    "ds_write_b128 v100, v[24:27] offset:55808\n"
    "ds_write_b128 v101, v[28:31] offset:55808\n"
    "s_waitcnt lgkmcnt(8)\n"
    "v_mfma_f32_16x16x32_bf16 v[32:35], v[232:235], v[60:63], v[32:35]\n"
    "ds_read_b128 v[56:59], v107 offset:2048\n"
    "v_mfma_f32_16x16x32_bf16 v[36:39], v[236:239], v[60:63], v[36:39]\n"
    "s_waitcnt lgkmcnt(4)\n"
    "v_mfma_f32_16x16x32_bf16 v[40:43], v[208:211], v[48:51], 0\n"
    "ds_read_b128 v[60:63], v107 offset:3072\n"
    "v_mfma_f32_16x16x32_bf16 v[44:47], v[212:215], v[48:51], 0\n"
    "v_add_f32 v136, v136, v140\n"
    "v_add_f32 v137, v137, v141\n"
    "v_add_f32 v138, v138, v142\n"
    "v_add_f32 v139, v139, v143\n"
    "v_add_f32 v144, v144, v148\n"
    "v_add_f32 v145, v145, v149\n"
    "v_add_f32 v146, v146, v150\n"
    "s_waitcnt lgkmcnt(4)\n"
    "v_mfma_f32_16x16x32_bf16 v[40:43], v[216:219], v[52:55], v[40:43]\n"
    "v_mfma_f32_16x16x32_bf16 v[44:47], v[220:223], v[52:55], v[44:47]\n"
    "v_add_f32 v147, v147, v151\n"
    "v_add_f32 v136, v136, v144\n"
    "v_add_f32 v137, v137, v145\n"
    "v_add_f32 v138, v138, v146\n"
    "v_add_f32 v139, v139, v147\n"
    "v_cvt_pk_bf16_f32 v140, v136, v137\n"
    "v_cvt_pk_bf16_f32 v141, v138, v139\n"
    "s_waitcnt lgkmcnt(1)\n"
    "v_mfma_f32_16x16x32_bf16 v[40:43], v[224:227], v[56:59], v[40:43]\n"
    "v_mfma_f32_16x16x32_bf16 v[44:47], v[228:231], v[56:59], v[44:47]\n"
    "ds_write_b128 v102, v[32:35] offset:55808\n"
    "ds_write_b128 v103, v[36:39] offset:55808\n"
    "v_add_u32 v102, 16384, v102\n"
    "v_add_u32 v103, 16384, v103\n"
    "v_lshlrev_b32 v136, 16, v140\n"
    "v_and_b32 v137, 0xffff0000, v140\n"
    "v_lshlrev_b32 v138, 16, v141\n"
    "v_and_b32 v139, 0xffff0000, v141\n"
    "global_store_dwordx4 v106, v[136:139], s[58:59] sc1\n"
    "s_add_u32 s58, s58, 147456\n"
    "s_addc_u32 s59, s59, 0\n"
    "s_waitcnt lgkmcnt(2)\n"
    "v_mfma_f32_16x16x32_bf16 v[40:43], v[232:235], v[60:63], v[40:43]\n"
    "v_mfma_f32_16x16x32_bf16 v[44:47], v[236:239], v[60:63], v[44:47]\n"
    "s_nop 6\n"
    "31:\n"
    "ds_write_b128 v102, v[40:43] offset:55808\n"
    "ds_write_b128 v103, v[44:47] offset:55808\n"
    "s_waitcnt lgkmcnt(0)\n"
    "s_barrier\n"
    "ds_read_b128 v[152:155], v104 offset:37120\n"
    "ds_read_b128 v[156:159], v104 offset:41216\n"
    "ds_read_b128 v[160:163], v104 offset:45312\n"
    "ds_read_b128 v[164:167], v104 offset:49408\n"
    "ds_read_b128 v[168:171], v105 offset:0\n"
    "ds_read_b128 v[172:175], v105 offset:4096\n"
    "ds_read_b128 v[176:179], v105 offset:8192\n"
    "ds_read_b128 v[180:183], v105 offset:12288\n"
    "s_waitcnt lgkmcnt(4)\n"
    "v_pk_add_f32 v[152:153], v[152:153], v[156:157]\n"
    "v_pk_add_f32 v[154:155], v[154:155], v[158:159]\n"
    "v_pk_add_f32 v[160:161], v[160:161], v[164:165]\n"
    "v_pk_add_f32 v[162:163], v[162:163], v[166:167]\n"
    "v_pk_add_f32 v[152:153], v[152:153], v[160:161]\n"
    "v_pk_add_f32 v[154:155], v[154:155], v[162:163]\n"
    "v_cvt_pk_bf16_f32 v156, v152, v153\n"
    "v_cvt_pk_bf16_f32 v157, v154, v155\n"
    "v_lshlrev_b32 v152, 16, v156\n"
    "v_and_b32 v153, 0xffff0000, v156\n"
    "v_lshlrev_b32 v154, 16, v157\n"
    "v_and_b32 v155, 0xffff0000, v157\n"
    "global_store_dwordx4 v106, v[152:155], s[58:59] sc1\n"
    "s_add_u32 s58, s58, s61\n"
    "s_addc_u32 s59, s59, 0\n"
    "s_waitcnt lgkmcnt(0)\n"
    "v_pk_add_f32 v[168:169], v[168:169], v[172:173]\n"
    "v_pk_add_f32 v[170:171], v[170:171], v[174:175]\n"
    "v_pk_add_f32 v[176:177], v[176:177], v[180:181]\n"
    "v_pk_add_f32 v[178:179], v[178:179], v[182:183]\n"
    "v_pk_add_f32 v[168:169], v[168:169], v[176:177]\n"
    "v_pk_add_f32 v[170:171], v[170:171], v[178:179]\n"
    "v_cvt_pk_bf16_f32 v172, v168, v169\n"
    "v_cvt_pk_bf16_f32 v173, v170, v171\n"
    "v_lshlrev_b32 v168, 16, v172\n"
    "v_and_b32 v169, 0xffff0000, v172\n"
    "v_lshlrev_b32 v170, 16, v173\n"
    "v_and_b32 v171, 0xffff0000, v173\n"
    "s_mov_b32 exec_hi, 0\n"
    "global_store_dwordx4 v106, v[168:171], s[58:59] sc1\n"
    "s_mov_b32 exec_hi, -1\n"
        : "+{s[42:43]}"(ogb), "+{s[44:45]}"(hp), "+{s[50:51]}"(xsk), "+{s[56:57]}"(outb), "+{s[58:59]}"(outa),
          "+{v[136:167]}"(uu), "+{v[168:199]}"(cf1), [cnt] "=&s"(cnt)
        : "{v[200:207]}"(cf2), "{v[208:239]}"(sp), "{v[240:243]}"(wf1), "{v[128:135]}"(mi), "{v[244:251]}"(ta),
          "{v[0:7]}"(tb), "{s[46:47]}"(tag), [fo] "s"(fo_off), [lq] "s"(lq), [w] "s"(w), [kh] "s"(kh), [dsk] "s"(P.dsk + ct3 * 128), [wx] "s"(ws + wfo + 26624), [ws] "s"(ws)
        : "memory", "scc", "vcc", "v8", "v9", "v10", "v11", "v12", "v13", "v14", "v15", "v16", "v17", "v18", "v19", "v20", "v21", "v22", "v23", "v24", "v25", "v26", "v27", "v28", "v29", "v30", "v31", "v32", "v33", "v34", "v35", "v36", "v37", "v38", "v39", "v40", "v41", "v42", "v43", "v44", "v45", "v46", "v47", "v48", "v49", "v50", "v51", "v52", "v53", "v54", "v55", "v56", "v57", "v58", "v59", "v60", "v61", "v62", "v63", "v64", "v65", "v66", "v67", "v68", "v69", "v70", "v71", "v72", "v73", "v74", "v75", "v76", "v77", "v78", "v79", "v80", "v81", "v82", "v83", "v84", "v85", "v86", "v87", "v88", "v89", "v90", "v91", "v92", "v93", "v94", "v95", "v96", "v97", "v98", "v99", "v100", "v101", "v102", "v103", "v104", "v105", "v106", "v107", "v108", "v109", "v110", "v111", "v112", "v113", "v114", "v115", "v252", "v253", "v254", "s40", "s41", "s48", "s49", "s52", "s53", "s54", "s55", "s60", "s61", "s62", "s63");
  }
  asm volatile(".Lcode_to:");
  if (un == 233 && w == 0 && lane == 0) st8(ws, WS_EP, tag);
}

extern "C" void kernel_launch(void* const* d_in, const int* in_sizes, int n_in, void* d_out, int out_size, void* d_ws,
                              size_t ws_size, hipStream_t stream) {
  Params P;
  P.xa = (const char*)d_in[0]; P.wqkv = (const char*)d_in[1]; P.wo = (const char*)d_in[2]; P.xb = (const char*)d_in[3];
  P.lre = (const char*)d_in[4]; P.lim = (const char*)d_in[5]; P.bre = (const char*)d_in[6]; P.bim = (const char*)d_in[7];
  P.cre = (const char*)d_in[8]; P.cim = (const char*)d_in[9]; P.ldt = (const char*)d_in[10]; P.dsk = (const char*)d_in[11];
  P.out = (float*)d_out; P.ws = (char*)d_ws;
  (void)hipFuncSetAttribute((const void*)fused, hipFuncAttributeMaxDynamicSharedMemorySize, LDS_BYTES);
  fused<<<256, 512, LDS_BYTES, stream>>>(P);
}
```
